```python
import torch

def _softmax(x, axis=-1):
    return torch.softmax(x, dim=axis)

def _gelu(x):
    return torch.nn.functional.gelu(x, approximate='tanh')

def _silu(x):
    return torch.nn.functional.silu(x)

def _sigmoid(x):
    return torch.sigmoid(x)

def _layernorm(x, axis=-1, eps=1e-05):
    m = x.mean(dim=axis, keepdim=True)
    v = x.var(dim=axis, keepdim=True, unbiased=False)
    return (x - m) / torch.sqrt(v + eps)

def _scatter_add(tgt, idx, src, axis=0):
    return tgt.clone().index_add_(axis, idx, src)

def _segment_sum(src, seg, n):
    out = torch.zeros((n,) + src.shape[1:], dtype=src.dtype)
    return out.index_add_(0, seg, src)

def _pad(x, pw):
    flat = tuple((v for pair in reversed(pw) for v in pair))
    return torch.nn.functional.pad(x, flat)
kernel_name = 'adaLN_cond__sliding_block__moe_arch_mod__prompt'

def setup_inputs():

    _w = lambda a: (lambda t: t.to(torch.bfloat16) if t.is_floating_point() else t)(torch.as_tensor(a))

    def _A_setup():
        B, S, D = (1, 2048, 512)
        return {'x': _w((lambda *s: torch.randn(*s, dtype=torch.bfloat16) * 0.05)(B, S, D)), 'wqkv': _w((lambda *s: torch.randn(*s, dtype=torch.bfloat16) * 0.05)(D, 3 * D)), 'wo': _w((lambda *s: torch.randn(*s, dtype=torch.bfloat16) * 0.05)(D, D))}
    _A_in = _A_setup()

    def _B_setup():
        B, S, D, F, C = (1, 2048, 512, 1792, 2)
        return {'x': _w((lambda *s: torch.randn(*s, dtype=torch.bfloat16) * 0.05)(B, S, D)), 'w_r': _w((lambda *s: torch.randn(*s, dtype=torch.bfloat16) * 0.05)(D)), 'w_gate': _w((lambda *s: torch.randn(*s, dtype=torch.bfloat16) * 0.05)(D, F)), 'w_up': _w((lambda *s: torch.randn(*s, dtype=torch.bfloat16) * 0.05)(D, F)), 'w_down': _w((lambda *s: torch.randn(*s, dtype=torch.bfloat16) * 0.05)(F, D))}
    _B_in = _B_setup()
    return {'A_x': _A_in['x'], 'A_wqkv': _A_in['wqkv'], 'A_wo': _A_in['wo'], 'B_x': _B_in['x'], 'B_w_r': _B_in['w_r'], 'B_w_gate': _B_in['w_gate'], 'B_w_up': _B_in['w_up'], 'B_w_down': _B_in['w_down'], 'c': _w((lambda *s: torch.randn(*s, dtype=torch.bfloat16) * 0.1)(1, 512) * 8.0), 'w_mod': _w((lambda *s: torch.randn(*s, dtype=torch.bfloat16) * 0.1)(512, 6 * 512) * 512 ** (-0.5) * 8.0)}

def reference(A_x, A_wqkv, A_wo, B_x, B_w_r, B_w_gate, B_w_up, B_w_down, c, w_mod):

    def _A_ref(x, wqkv, wo):
        B, S, D = x.shape
        H = 32
        Dh = D // H
        W = 256
        qkv = (lambda x, *d: x.permute(*d))((x @ wqkv).reshape(B, S, 3, H, Dh), 2, 0, 3, 1, 4)
        q, k, v = (qkv[0], qkv[1], qkv[2])
        scores = torch.einsum('bhqd,bhkd->bhqk', q, k) * Dh ** (-0.5)
        idx = torch.arange(S)
        mask = (idx[:, None] - idx[None, :] >= 0) & (idx[:, None] - idx[None, :] < W)
        scores = torch.where(mask[None, None], scores, -1000000000.0)
        p = _softmax(scores, axis=-1)
        o = torch.einsum('bhqk,bhkd->bhqd', p, v)
        return (lambda x, *d: x.permute(*d))(o, 0, 2, 1, 3).reshape(B, S, D) @ wo

    def _B_ref(x, w_r, w_gate, w_up, w_down):
        B, S, D = x.shape
        C = 2
        r = x @ w_r
        idx = torch.argsort(-r, axis=-1)[:, :C]
        idx_e = (lambda x, n, axis: torch.repeat_interleave(x, n, dim=axis))(idx.reshape(B, C, 1), D, axis=2)
        x_sel = (lambda x, i, axis: torch.take_along_dim(x, i, dim=axis))(x, idx_e, axis=1)
        g = _silu(x_sel @ w_gate)
        u = x_sel @ w_up
        y = g * u @ w_down
        r_sel = _sigmoid((lambda x, i, axis: torch.take_along_dim(x, i, dim=axis))(r, idx, axis=1))
        y = y * r_sel.reshape(B, C, 1)
        b_off = (torch.arange(B) * S).reshape(B, 1)
        flat_idx = (idx + b_off).reshape(B * C)
        zero = (x * 0.0).reshape(B * S, D)
        upd = _scatter_add(zero, flat_idx, y.reshape(B * C, D), axis=0)
        return x + upd.reshape(B, S, D)
    x = A_x
    mod = (_silu(c) @ w_mod).reshape(c.shape[0], 6, 1, -1)
    sa, ca, ga, sb, cb, gb = (mod[:, 0].clone(), mod[:, 1].clone(), mod[:, 2].clone(), mod[:, 3].clone(), mod[:, 4].clone(), mod[:, 5].clone())
    h = _layernorm(x, axis=-1) * (1 + ca) + sa
    x = x + ga * _A_ref(h, A_wqkv, A_wo)
    h = _layernorm(x, axis=-1) * (1 + cb) + sb
    x = x + gb * _B_ref(h, B_w_r, B_w_gate, B_w_up, B_w_down)
    return x
```

```cpp
#include <hip/hip_runtime.h>
#include <hip/hip_bf16.h>
typedef unsigned short u16;
typedef float f32x4 __attribute__((ext_vector_type(4)));
typedef __bf16 bf8 __attribute__((ext_vector_type(8)));
typedef unsigned v4u __attribute__((ext_vector_type(4)));
#define GAS __attribute__((address_space(1)))
#define LAS __attribute__((address_space(3)))
__device__ __forceinline__ float b2f(u16 v) { return __uint_as_float((unsigned)v << 16); }
__device__ __forceinline__ u16 f2b(float f) { __bf16 b = (__bf16)f; return *(u16*)&b; }
__device__ __forceinline__ float rb(float f) { return b2f(f2b(f)); }
__device__ __forceinline__ float wsum(float v) { for (int m = 32; m >= 1; m >>= 1) v += __shfl_xor(v, m); return v; }

__global__ void k_mod(const u16* c, const u16* wm, float* mod) {
    __shared__ float red[4][64];
    int t = threadIdx.x, col = blockIdx.x * 64 + (t & 63), g = t >> 6;
    float a = 0;
    for (int k = g * 128; k < g * 128 + 128; ++k) { float cv = b2f(c[k]); float s = rb(cv / (1.f + __expf(-cv))); a += s * b2f(wm[k * 3072 + col]); }
    red[g][t & 63] = a; __syncthreads();
    if (t < 64) mod[col] = rb(red[0][t] + red[1][t] + red[2][t] + red[3][t]);
}
__global__ void k_ln1(const u16* x, const float* mod, u16* h) {
    int tok = blockIdx.x * 4 + (threadIdx.x >> 6), l = threadIdx.x & 63;
    float v[8]; float s = 0;
    for (int j = 0; j < 8; ++j) { v[j] = b2f(x[tok * 512 + l * 8 + j]); s += v[j]; }
    float m = wsum(s) / 512; float q = 0;
    for (int j = 0; j < 8; ++j) q += (v[j] - m) * (v[j] - m);
    float rs = rsqrtf(wsum(q) / 512 + 1e-5f);
    for (int j = 0; j < 8; ++j) { int k = l * 8 + j; h[tok * 512 + k] = f2b((v[j] - m) * rs * rb(1.f + mod[512 + k]) + mod[k]); }
}
__global__ void k_gemm(const u16* A, const u16* B, u16* C, int N) {
    __shared__ __attribute__((aligned(16))) u16 As[64][72], Bs[64][72];
    int t = threadIdx.x, w = t >> 6, l = t & 63, m0 = blockIdx.y * 64, n0 = blockIdx.x * 64;
    f32x4 acc[4] = {};
    for (int k0 = 0; k0 < 512; k0 += 64) {
        for (int i = 0; i < 2; ++i) {
            int r = i * 32 + (t >> 3), c8 = (t & 7) * 8;
            *(v4u*)&As[r][c8] = *(const v4u*)&A[(m0 + r) * 512 + k0 + c8];
            v4u b = *(const v4u*)&B[(k0 + r) * N + n0 + c8];
            const u16* bp = (const u16*)&b;
            for (int j = 0; j < 8; ++j) Bs[c8 + j][r] = bp[j];
        }
        __syncthreads();
        for (int s = 0; s < 2; ++s) {
            bf8 a = *(bf8*)&As[w * 16 + (l & 15)][s * 32 + (l >> 4) * 8];
            for (int n = 0; n < 4; ++n) { bf8 b = *(bf8*)&Bs[n * 16 + (l & 15)][s * 32 + (l >> 4) * 8]; acc[n] = __builtin_amdgcn_mfma_f32_16x16x32_bf16(a, b, acc[n], 0, 0, 0); }
        }
        __syncthreads();
    }
    for (int n = 0; n < 4; ++n) for (int r = 0; r < 4; ++r) C[(m0 + w * 16 + (l >> 4) * 4 + r) * N + n0 + n * 16 + (l & 15)] = f2b(acc[n][r]);
}
__global__ void k_attn(const u16* qkv, u16* o) {
    __shared__ float Ks[320][17], Vs[320][17];
    int h = blockIdx.y, q0 = blockIdx.x * 64, t = threadIdx.x;
    for (int i = t; i < 320 * 16; i += 256) { int r = i >> 4, d = i & 15, tok = q0 - 256 + r; Ks[r][d] = tok >= 0 ? b2f(qkv[tok * 1536 + 512 + h * 16 + d]) : 0.f; Vs[r][d] = tok >= 0 ? b2f(qkv[tok * 1536 + 1024 + h * 16 + d]) : 0.f; }
    __syncthreads();
    int qi = t >> 2, part = t & 3, q = q0 + qi;
    float qv[16]; for (int d = 0; d < 16; ++d) qv[d] = b2f(qkv[q * 1536 + h * 16 + d]) * 0.25f;
    float m = -1e30f, lsum = 0, acc[16] = {};
    for (int j = part; j < 256; j += 4) {
        int r = qi + 256 - j; if (q - j < 0) break;
        float s = 0; for (int d = 0; d < 16; ++d) s += qv[d] * Ks[r][d];
        float mn = fmaxf(m, s), a = __expf(m - mn), p = __expf(s - mn);
        lsum = lsum * a + p; for (int d = 0; d < 16; ++d) acc[d] = acc[d] * a + p * Vs[r][d]; m = mn;
    }
    float mg = fmaxf(m, __shfl_xor(m, 1)); mg = fmaxf(mg, __shfl_xor(mg, 2));
    float a = __expf(m - mg); lsum *= a; lsum += __shfl_xor(lsum, 1); lsum += __shfl_xor(lsum, 2);
    for (int d = 0; d < 16; ++d) { float v = acc[d] * a; v += __shfl_xor(v, 1); v += __shfl_xor(v, 2); if (part == 0) o[q * 512 + h * 16 + d] = f2b(v / lsum); }
}
__global__ void k_ln2(const u16* x, const u16* y, const float* mod, const u16* wr, float* x1o, u16* h2, float* r, u16* out) {
    int tok = blockIdx.x * 4 + (threadIdx.x >> 6), l = threadIdx.x & 63;
    float v[8]; float s = 0;
    for (int j = 0; j < 8; ++j) { int k = l * 8 + j; v[j] = rb(b2f(x[tok * 512 + k]) + rb(mod[1024 + k] * b2f(y[tok * 512 + k]))); s += v[j]; }
    float m = wsum(s) / 512; float q = 0;
    for (int j = 0; j < 8; ++j) q += (v[j] - m) * (v[j] - m);
    float rs = rsqrtf(wsum(q) / 512 + 1e-5f); float rr = 0;
    for (int j = 0; j < 8; ++j) { int k = l * 8 + j; float hh = rb((v[j] - m) * rs * rb(1.f + mod[2048 + k]) + mod[1536 + k]); h2[tok * 512 + k] = f2b(hh); x1o[tok * 512 + k] = v[j]; rr += hh * b2f(wr[k]); out[tok * 512 + k] = f2b(v[j] + mod[2560 + k] * hh); }
    rr = wsum(rr); if (l == 0) r[tok] = rr;
}
__device__ void top2(const float* r, int& i0, int& i1, unsigned long long* sm) {
    int t = threadIdx.x; unsigned long long best[2];
    for (int pass = 0; pass < 2; ++pass) {
        unsigned long long b = 0;
        for (int i = t; i < 2048; i += 256) { unsigned u = __float_as_uint(r[i]); u = (u >> 31) ? ~u : (u | 0x80000000u); unsigned long long key = ((unsigned long long)u << 32) | (2047 - i); if (pass == 1 && key == best[0]) key = 0; b = key > b ? key : b; }
        for (int m = 32; m >= 1; m >>= 1) { unsigned long long o = __shfl_xor(b, m); b = o > b ? o : b; }
        __syncthreads(); if ((t & 63) == 0) sm[t >> 6] = b; __syncthreads();
        b = sm[0]; for (int i = 1; i < 4; ++i) b = sm[i] > b ? sm[i] : b; best[pass] = b;
    }
    i0 = 2047 - (int)(best[0] & 2047); i1 = 2047 - (int)(best[1] & 2047);
}
__global__ void k_gu(const float* r, const u16* h2, const u16* wg, const u16* wu, float* gu) {
    __shared__ unsigned long long sm[4]; __shared__ float red[4][4][64];
    int i0, i1; top2(r, i0, i1, sm);
    int t = threadIdx.x, col = blockIdx.x * 64 + (t & 63), g = t >> 6;
    float a0 = 0, a1 = 0, b0 = 0, b1 = 0;
    for (int k = g * 128; k < g * 128 + 128; ++k) { float x0 = b2f(h2[i0 * 512 + k]), x1 = b2f(h2[i1 * 512 + k]), wgv = b2f(wg[k * 1792 + col]), wuv = b2f(wu[k * 1792 + col]); a0 += x0 * wgv; a1 += x1 * wgv; b0 += x0 * wuv; b1 += x1 * wuv; }
    red[0][g][t & 63] = a0; red[1][g][t & 63] = a1; red[2][g][t & 63] = b0; red[3][g][t & 63] = b1; __syncthreads();
    if (t < 128) { int s = t >> 6, c = t & 63; float gg = 0, uu = 0; for (int i = 0; i < 4; ++i) { gg += red[s][i][c]; uu += red[2 + s][i][c]; } gg = rb(gg); gu[s * 1792 + col] = rb(rb(gg / (1.f + __expf(-gg))) * rb(uu)); }
}
__global__ void k_down(const float* r, const float* gu, const u16* wd, const float* x1, const u16* h2, const float* mod, u16* out) {
    __shared__ unsigned long long sm[4]; __shared__ float red[2][16][16];
    int idx[2]; top2(r, idx[0], idx[1], sm);
    int t = threadIdx.x, col = blockIdx.x * 16 + (t & 15), g = t >> 4;
    float a0 = 0, a1 = 0;
    for (int k = g * 112; k < g * 112 + 112; ++k) { float w = b2f(wd[k * 512 + col]); a0 += gu[k] * w; a1 += gu[1792 + k] * w; }
    red[0][g][t & 15] = a0; red[1][g][t & 15] = a1; __syncthreads();
    if (t < 32) { int s = t >> 4; float y = 0; for (int i = 0; i < 16; ++i) y += red[s][i][t & 15]; int tok = idx[s]; float sg = 1.f / (1.f + __expf(-r[tok])); out[tok * 512 + col] = f2b(x1[tok * 512 + col] + mod[2560 + col] * (b2f(h2[tok * 512 + col]) + y * sg)); }
}
#define PROBE 0
__global__ void __launch_bounds__(512, 2) k_probe(const char* x, unsigned* outp) {
    extern __shared__ char lds[];
}
extern "C" void kernel_launch(void* const* d_in, const int* in_sizes, int n_in, void* d_out, int out_size, void* d_ws, size_t ws_size, hipStream_t stream) {
    const u16 *x = (const u16*)d_in[0], *wqkv = (const u16*)d_in[1], *wo = (const u16*)d_in[2], *wr = (const u16*)d_in[4], *wg = (const u16*)d_in[5], *wu = (const u16*)d_in[6], *wd = (const u16*)d_in[7], *c = (const u16*)d_in[8], *wm = (const u16*)d_in[9];
    char* ws = (char*)d_ws;
    float* mod = (float*)ws; float* r = (float*)(ws + 16384); float* gu = (float*)(ws + 32768);
    u16* h = (u16*)(ws + (1 << 20)); u16* qkv = (u16*)(ws + (4 << 20)); u16* o = (u16*)(ws + (12 << 20)); u16* y = (u16*)(ws + (16 << 20)); float* x1 = (float*)(ws + (20 << 20)); u16* h2 = (u16*)(ws + (26 << 20));
    k_mod<<<48, 256, 0, stream>>>(c, wm, mod);
    k_ln1<<<512, 256, 0, stream>>>(x, mod, h);
    k_gemm<<<dim3(24, 32), 256, 0, stream>>>(h, wqkv, qkv, 1536);
    k_attn<<<dim3(32, 32), 256, 0, stream>>>(qkv, o);
    k_gemm<<<dim3(8, 32), 256, 0, stream>>>(o, wo, y, 512);
    k_ln2<<<512, 256, 0, stream>>>(x, y, mod, wr, x1, h2, r, (u16*)d_out);
    k_gu<<<28, 256, 0, stream>>>(r, h2, wg, wu, gu);
    k_down<<<32, 256, 0, stream>>>(r, gu, wd, x1, h2, mod, (u16*)d_out);
}
```

```cpp
#define PROBE 9
#define USEMAX 0
#include <hip/hip_runtime.h>
#include <hip/hip_bf16.h>
typedef unsigned short u16;
typedef float f32x4 __attribute__((ext_vector_type(4)));
typedef __bf16 bf8 __attribute__((ext_vector_type(8)));
typedef unsigned v4u __attribute__((ext_vector_type(4)));
#define GAS __attribute__((address_space(1)))
#define LAS __attribute__((address_space(3)))
__device__ __forceinline__ float b2f(u16 v) { return __uint_as_float((unsigned)v << 16); }
__device__ __forceinline__ u16 f2b(float f) { __bf16 b = (__bf16)f; return *(u16*)&b; }
__device__ __forceinline__ float rb(float f) { return b2f(f2b(f)); }
__device__ __forceinline__ float wsum(float v) { for (int m = 32; m >= 1; m >>= 1) v += __shfl_xor(v, m); return v; }

__global__ void k_mod(const u16* c, const u16* wm, float* mod) {
    __shared__ float red[4][64];
    int t = threadIdx.x, col = blockIdx.x * 64 + (t & 63), g = t >> 6;
    float a = 0;
    for (int k = g * 128; k < g * 128 + 128; ++k) { float cv = b2f(c[k]); float s = rb(cv / (1.f + __expf(-cv))); a += s * b2f(wm[k * 3072 + col]); }
    red[g][t & 63] = a; __syncthreads();
    if (t < 64) mod[col] = rb(red[0][t] + red[1][t] + red[2][t] + red[3][t]);
}
__global__ void k_ln1(const u16* x, const float* mod, u16* h) {
    int tok = blockIdx.x * 4 + (threadIdx.x >> 6), l = threadIdx.x & 63;
    float v[8]; float s = 0;
    for (int j = 0; j < 8; ++j) { v[j] = b2f(x[tok * 512 + l * 8 + j]); s += v[j]; }
    float m = wsum(s) / 512; float q = 0;
    for (int j = 0; j < 8; ++j) q += (v[j] - m) * (v[j] - m);
    float rs = rsqrtf(wsum(q) / 512 + 1e-5f);
    for (int j = 0; j < 8; ++j) { int k = l * 8 + j; h[tok * 512 + k] = f2b((v[j] - m) * rs * rb(1.f + mod[512 + k]) + mod[k]); }
}
__global__ void k_gemm(const u16* A, const u16* B, u16* C, int N) {
    __shared__ __attribute__((aligned(16))) u16 As[64][72], Bs[64][72];
    int t = threadIdx.x, w = t >> 6, l = t & 63, m0 = blockIdx.y * 64, n0 = blockIdx.x * 64;
    f32x4 acc[4] = {};
    for (int k0 = 0; k0 < 512; k0 += 64) {
        for (int i = 0; i < 2; ++i) {
            int r = i * 32 + (t >> 3), c8 = (t & 7) * 8;
            *(v4u*)&As[r][c8] = *(const v4u*)&A[(m0 + r) * 512 + k0 + c8];
            v4u b = *(const v4u*)&B[(k0 + r) * N + n0 + c8];
            const u16* bp = (const u16*)&b;
            for (int j = 0; j < 8; ++j) Bs[c8 + j][r] = bp[j];
        }
        __syncthreads();
        for (int s = 0; s < 2; ++s) {
            bf8 a = *(bf8*)&As[w * 16 + (l & 15)][s * 32 + (l >> 4) * 8];
            for (int n = 0; n < 4; ++n) { bf8 b = *(bf8*)&Bs[n * 16 + (l & 15)][s * 32 + (l >> 4) * 8]; acc[n] = __builtin_amdgcn_mfma_f32_16x16x32_bf16(a, b, acc[n], 0, 0, 0); }
        }
        __syncthreads();
    }
    for (int n = 0; n < 4; ++n) for (int r = 0; r < 4; ++r) C[(m0 + w * 16 + (l >> 4) * 4 + r) * N + n0 + n * 16 + (l & 15)] = f2b(acc[n][r]);
}
__global__ void k_attn(const u16* qkv, u16* o) {
    __shared__ float Ks[320][17], Vs[320][17];
    int h = blockIdx.y, q0 = blockIdx.x * 64, t = threadIdx.x;
    for (int i = t; i < 320 * 16; i += 256) { int r = i >> 4, d = i & 15, tok = q0 - 256 + r; Ks[r][d] = tok >= 0 ? b2f(qkv[tok * 1536 + 512 + h * 16 + d]) : 0.f; Vs[r][d] = tok >= 0 ? b2f(qkv[tok * 1536 + 1024 + h * 16 + d]) : 0.f; }
    __syncthreads();
    int qi = t >> 2, part = t & 3, q = q0 + qi;
    float qv[16]; for (int d = 0; d < 16; ++d) qv[d] = b2f(qkv[q * 1536 + h * 16 + d]) * 0.25f;
    float m = -1e30f, lsum = 0, acc[16] = {};
    for (int j = part; j < 256; j += 4) {
        int r = qi + 256 - j; if (q - j < 0) break;
        float s = 0; for (int d = 0; d < 16; ++d) s += qv[d] * Ks[r][d];
        float mn = fmaxf(m, s), a = __expf(m - mn), p = __expf(s - mn);
        lsum = lsum * a + p; for (int d = 0; d < 16; ++d) acc[d] = acc[d] * a + p * Vs[r][d]; m = mn;
    }
    float mg = fmaxf(m, __shfl_xor(m, 1)); mg = fmaxf(mg, __shfl_xor(mg, 2));
    float a = __expf(m - mg); lsum *= a; lsum += __shfl_xor(lsum, 1); lsum += __shfl_xor(lsum, 2);
    for (int d = 0; d < 16; ++d) { float v = acc[d] * a; v += __shfl_xor(v, 1); v += __shfl_xor(v, 2); if (part == 0) o[q * 512 + h * 16 + d] = f2b(v / lsum); }
}
__global__ void k_ln2(const u16* x, const u16* y, const float* mod, const u16* wr, float* x1o, u16* h2, float* r, u16* out) {
    int tok = blockIdx.x * 4 + (threadIdx.x >> 6), l = threadIdx.x & 63;
    float v[8]; float s = 0;
    for (int j = 0; j < 8; ++j) { int k = l * 8 + j; v[j] = rb(b2f(x[tok * 512 + k]) + rb(mod[1024 + k] * b2f(y[tok * 512 + k]))); s += v[j]; }
    float m = wsum(s) / 512; float q = 0;
    for (int j = 0; j < 8; ++j) q += (v[j] - m) * (v[j] - m);
    float rs = rsqrtf(wsum(q) / 512 + 1e-5f); float rr = 0;
    for (int j = 0; j < 8; ++j) { int k = l * 8 + j; float hh = rb((v[j] - m) * rs * rb(1.f + mod[2048 + k]) + mod[1536 + k]); h2[tok * 512 + k] = f2b(hh); x1o[tok * 512 + k] = v[j]; rr += hh * b2f(wr[k]); out[tok * 512 + k] = f2b(v[j] + mod[2560 + k] * hh); }
    rr = wsum(rr); if (l == 0) r[tok] = rr;
}
__device__ void top2(const float* r, int& i0, int& i1, unsigned long long* sm) {
    int t = threadIdx.x; unsigned long long best[2];
    for (int pass = 0; pass < 2; ++pass) {
        unsigned long long b = 0;
        for (int i = t; i < 2048; i += 256) { unsigned u = __float_as_uint(r[i]); u = (u >> 31) ? ~u : (u | 0x80000000u); unsigned long long key = ((unsigned long long)u << 32) | (2047 - i); if (pass == 1 && key == best[0]) key = 0; b = key > b ? key : b; }
        for (int m = 32; m >= 1; m >>= 1) { unsigned long long o = __shfl_xor(b, m); b = o > b ? o : b; }
        __syncthreads(); if ((t & 63) == 0) sm[t >> 6] = b; __syncthreads();
        b = sm[0]; for (int i = 1; i < 4; ++i) b = sm[i] > b ? sm[i] : b; best[pass] = b;
    }
    i0 = 2047 - (int)(best[0] & 2047); i1 = 2047 - (int)(best[1] & 2047);
}
__global__ void k_gu(const float* r, const u16* h2, const u16* wg, const u16* wu, float* gu) {
    __shared__ unsigned long long sm[4]; __shared__ float red[4][4][64];
    int i0, i1; top2(r, i0, i1, sm);
    int t = threadIdx.x, col = blockIdx.x * 64 + (t & 63), g = t >> 6;
    float a0 = 0, a1 = 0, b0 = 0, b1 = 0;
    for (int k = g * 128; k < g * 128 + 128; ++k) { float x0 = b2f(h2[i0 * 512 + k]), x1 = b2f(h2[i1 * 512 + k]), wgv = b2f(wg[k * 1792 + col]), wuv = b2f(wu[k * 1792 + col]); a0 += x0 * wgv; a1 += x1 * wgv; b0 += x0 * wuv; b1 += x1 * wuv; }
    red[0][g][t & 63] = a0; red[1][g][t & 63] = a1; red[2][g][t & 63] = b0; red[3][g][t & 63] = b1; __syncthreads();
    if (t < 128) { int s = t >> 6, c = t & 63; float gg = 0, uu = 0; for (int i = 0; i < 4; ++i) { gg += red[s][i][c]; uu += red[2 + s][i][c]; } gg = rb(gg); gu[s * 1792 + col] = rb(rb(gg / (1.f + __expf(-gg))) * rb(uu)); }
}
__global__ void k_down(const float* r, const float* gu, const u16* wd, const float* x1, const u16* h2, const float* mod, u16* out) {
    __shared__ unsigned long long sm[4]; __shared__ float red[2][16][16];
    int idx[2]; top2(r, idx[0], idx[1], sm);
    int t = threadIdx.x, col = blockIdx.x * 16 + (t & 15), g = t >> 4;
    float a0 = 0, a1 = 0;
    for (int k = g * 112; k < g * 112 + 112; ++k) { float w = b2f(wd[k * 512 + col]); a0 += gu[k] * w; a1 += gu[1792 + k] * w; }
    red[0][g][t & 15] = a0; red[1][g][t & 15] = a1; __syncthreads();
    if (t < 32) { int s = t >> 4; float y = 0; for (int i = 0; i < 16; ++i) y += red[s][i][t & 15]; int tok = idx[s]; float sg = 1.f / (1.f + __expf(-r[tok])); out[tok * 512 + col] = f2b(x1[tok * 512 + col] + mod[2560 + col] * (b2f(h2[tok * 512 + col]) + y * sg)); }
}

__device__ __forceinline__ unsigned long long rt() { __builtin_amdgcn_sched_barrier(0); unsigned long long t; asm volatile("s_memrealtime %0\n s_waitcnt lgkmcnt(0)" : "=s"(t) :: "memory"); __builtin_amdgcn_sched_barrier(0); return t; }
__global__ void __launch_bounds__(256, 1) k_probe(const char* x_, unsigned long long* st_, char* buf_) {
    extern __shared__ char lds[];
    unsigned long long ta, tb;
    const char* x; unsigned long long* st; char* buf;
    {
        auto kp = __builtin_amdgcn_kernarg_segment_ptr();
        asm volatile("s_memrealtime %0\n s_waitcnt lgkmcnt(0)\n s_load_dwordx2 %2, %5, 0\n s_load_dwordx2 %3, %5, 8\n s_load_dwordx2 %4, %5, 16\n s_waitcnt lgkmcnt(0)\n s_memrealtime %1\n s_waitcnt lgkmcnt(0)"
          : "=&s"(ta), "=&s"(tb), "=&s"(x), "=&s"(st), "=&s"(buf) : "s"(kp) : "memory");
    }
    const int b = blockIdx.x, lane = threadIdx.x & 63, w = __builtin_amdgcn_readfirstlane(threadIdx.x >> 6);
    unsigned long long t0 = rt(), t1 = 0;
    if (threadIdx.x == 0) { ((GAS unsigned long long*)st)[b * 2] = t0; ((GAS unsigned long long*)st)[b * 2 + 1] = t1; }
}
__global__ void k_spin(const unsigned long long* st) {
    int l = threadIdx.x; long long mx = 0, sum = 0; unsigned long long mn = ~0ull, mxs = 0;
    for (int i = l; i < 256; i += 64) { long long d = st[i * 2 + 1] - st[i * 2]; mx = d > mx ? d : mx; sum += d; mn = st[i*2] < mn ? st[i*2] : mn; mxs = st[i*2] > mxs ? st[i*2] : mxs; }
    for (int m = 32; m >= 1; m >>= 1) { long long o = __shfl_xor(mx, m); mx = o > mx ? o : mx; sum += __shfl_xor(sum, m); unsigned long long a = __shfl_xor(mn, m); mn = a < mn ? a : mn; a = __shfl_xor(mxs, m); mxs = a > mxs ? a : mxs; }
    long long metric = (PROBE == 5) ? (long long)(mxs - mn) * 100 : (PROBE == 9 ? 0 : (USEMAX ? mx * 100 : sum * 100 / 256));
    if (metric > 200000) metric = 200000;
    unsigned long long t0 = rt();
    while ((long long)(rt() - t0) < metric) {}
}
extern "C" void kernel_launch(void* const* d_in, const int* in_sizes, int n_in, void* d_out, int out_size, void* d_ws, size_t ws_size, hipStream_t stream) {
    const u16 *x = (const u16*)d_in[0], *wqkv = (const u16*)d_in[1], *wo = (const u16*)d_in[2], *wr = (const u16*)d_in[4], *wg = (const u16*)d_in[5], *wu = (const u16*)d_in[6], *wd = (const u16*)d_in[7], *c = (const u16*)d_in[8], *wm = (const u16*)d_in[9];
    char* ws = (char*)d_ws;
    float* mod = (float*)ws; float* r = (float*)(ws + 16384); float* gu = (float*)(ws + 32768);
    u16* h = (u16*)(ws + (1 << 20)); u16* qkv = (u16*)(ws + (4 << 20)); u16* o = (u16*)(ws + (12 << 20)); u16* y = (u16*)(ws + (16 << 20)); float* x1 = (float*)(ws + (20 << 20)); u16* h2 = (u16*)(ws + (26 << 20));
    hipFuncSetAttribute((const void*)k_probe, hipFuncAttributeMaxDynamicSharedMemorySize, 140 * 1024);
    k_probe<<<256, 256, 140 * 1024, stream>>>((const char*)d_in[3], (unsigned long long*)(ws + (30 << 20)), ws + (32 << 20));
    k_spin<<<1, 64, 0, stream>>>((const unsigned long long*)(ws + (30 << 20)));
    k_mod<<<48, 256, 0, stream>>>(c, wm, mod);
    k_ln1<<<512, 256, 0, stream>>>(x, mod, h);
    k_gemm<<<dim3(24, 32), 256, 0, stream>>>(h, wqkv, qkv, 1536);
    k_attn<<<dim3(32, 32), 256, 0, stream>>>(qkv, o);
    k_gemm<<<dim3(8, 32), 256, 0, stream>>>(o, wo, y, 512);
    k_ln2<<<512, 256, 0, stream>>>(x, y, mod, wr, x1, h2, r, (u16*)d_out);
    k_gu<<<28, 256, 0, stream>>>(r, h2, wg, wu, gu);
    k_down<<<32, 256, 0, stream>>>(r, gu, wd, x1, h2, mod, (u16*)d_out);
}
```

```cpp
#include <hip/hip_runtime.h>

struct Srd { const void* p; unsigned bytes; unsigned fmt; };
struct Args { Srd b[11]; };

__global__ void __launch_bounds__(512, 2) fused_block(Args) {
  asm volatile(
    ".Lk_Lstart:\n"
    "s_load_dwordx16 s[4:19], s[0:1], 0\n"
    "s_load_dwordx16 s[20:35], s[0:1], 64\n"
    "s_load_dwordx8 s[36:43], s[0:1], 128\n"
    "s_load_dwordx4 s[44:47], s[0:1], 160\n"
    "s_getpc_b64 s[60:61]\n"
    ".Lk_Lpc:\n"
    "s_lshr_b32 s57, s2, 3\n"
    "s_and_b32 s59, s57, 7\n"
    "v_lshl_add_u32 v2, v0, 3, s59\n"
    "v_lshlrev_b32 v2, 7, v2\n"
    "v_cmp_gt_u32 vcc, .Lk_Lend-.Lk_Lpc-4, v2\n"
    "s_nop 0\n"
    "s_and_saveexec_b64 s[62:63], vcc\n"
    "s_cbranch_execz .Lk_Lnopf\n"
    "global_load_dword v48, v2, s[60:61]\n"
    ".Lk_Lnopf:\n"
    "s_mov_b64 exec, -1\n"
    "v_readfirstlane_b32 s61, v0\n"
    "v_and_b32 v0, 255, v0\n"
    "v_lshrrev_b32 v19, 3, v0\n"
    "v_and_b32_e64 v18, 7, v0\n"
    "s_and_b32 s56, s2, 7\n"
    "s_lshr_b32 s58, s56, 1\n"
    "s_lshr_b32 s59, s57, 2\n"
    "s_lshl3_add_u32 s49, s58, s59\n"
    "s_and_b32 s58, s56, 1\n"
    "s_and_b32 s59, s57, 3\n"
    "s_lshl2_add_u32 s50, s58, s59\n"
    "s_lshl1_add_u32 s52, s49, s58\n"
    "s_add_u32 s51, s49, s59\n"
    "s_and_b32 s51, s51, 1\n"
    "s_bfe_u32 s48, s61, 0x20006\n"
    "s_and_b32 s58, s2, 3\n"
    "s_lshl_b32 s60, s58, 7\n"
    "v_lshl_add_u32 v16, v19, 2, s60\n"
    "s_lshr_b32 s59, s2, 2\n"
    "s_lshl_b32 s59, s59, 7\n"
    "s_mul_i32 s58, s58, 12288\n"
    "s_lshl1_add_u32 s58, s59, s58\n"
    "s_add_u32 s58, s58, 16384\n"
    "s_cmp_lt_u32 s2, 192\n"
    "s_cselect_b32 s59, s59, 0x7f000000\n"
    "s_cselect_b32 s58, s58, 0x7f000000\n"
    "v_mul_u32_u24 v17, 6144, v16\n"
    "v_lshl_add_u32 v20, v18, 4, s59\n"
    "v_add_u32 v8, v17, v20\n"
    "v_lshlrev_b32_e64 v16, 1, v16\n"
    "v_lshl_add_u32 v9, v0, 4, s58\n"
    "s_movk_i32 s64, 6144\n"
    "s_movk_i32 s65, 12288\n"
    "s_movk_i32 s69, 18432\n"
    "s_cmp_lt_u32 s2, 64\n"
    "s_cselect_b32 s58, 0, 0x7f000000\n"
    "v_or_b32_e64 v21, s58, v8\n"
    "s_lshl2_add_u32 s58, s49, s48\n"
    "s_lshl_b32 s58, s58, 14\n"
    "v_bfe_u32 v22, v0, 5, 1\n"
    "v_lshl_add_u32 v22, v22, 10, s58\n"
    "v_and_b32_e64 v23, 31, v0\n"
    "v_lshl_add_u32 v14, v23, 4, v22\n"
    "s_and_b32 s58, s50, 2\n"
    "s_lshr_b32 s59, s50, 2\n"
    "s_add_u32 s58, s58, s59\n"
    "s_lshl_b32 s66, s58, 1\n"
    "s_lshl2_add_u32 s67, s66, s51\n"
    "s_lshl_b32 s67, s67, 9\n"
    "s_bitcmp1_b32 s61, 8\n"
    "s_waitcnt lgkmcnt(0)\n"
    "s_cbranch_scc0 .Lk_LroleB\n"
    "s_xor_b32 s68, s67, 0\n"
    "buffer_load_dwordx4 v[64:67], v14, s[12:15], s68 offen\n"
    "s_xor_b32 s68, s67, 2048\n"
    "buffer_load_dwordx4 v[68:71], v14, s[12:15], s68 offen\n"
    "s_xor_b32 s68, s67, 4096\n"
    "buffer_load_dwordx4 v[72:75], v14, s[12:15], s68 offen\n"
    "s_xor_b32 s68, s67, 6144\n"
    "buffer_load_dwordx4 v[76:79], v14, s[12:15], s68 offen\n"
    "s_xor_b32 s68, s67, 8192\n"
    "buffer_load_dwordx4 v[80:83], v14, s[12:15], s68 offen\n"
    "s_xor_b32 s68, s67, 10240\n"
    "buffer_load_dwordx4 v[84:87], v14, s[12:15], s68 offen\n"
    "buffer_load_dword v6, off, s[20:23], 0 sc1\n"
    "s_branch .Lk_LroleA\n"
    ".Lk_LroleB:\n"
    "s_setprio 2\n"
    "buffer_load_dwordx2 v[62:63], v16, s[8:11], 0 offen\n"
    "buffer_load_dword v69, off, s[20:23], 0 sc1\n"
    "buffer_load_dwordx4 v[112:115], v21, s[4:7], 0 offen\n"
    "buffer_load_dwordx4 v[116:119], v21, s[4:7], s64 offen\n"
    "buffer_load_dwordx4 v[120:123], v21, s[4:7], s65 offen\n"
    "buffer_load_dwordx4 v[124:127], v21, s[4:7], s69 offen\n"
    "v_and_b32 v1, 63, v0\n"
    "v_and_b32 v2, 31, v0\n"
    "v_bfe_u32 v12, v0, 5, 1\n"
    "v_lshlrev_b32_e64 v13, 4, v1\n"
    "s_add_u32 s54, s56, s57\n"
    "s_and_b32 s58, s57, 7\n"
    "s_cselect_b32 s54, 99, s54\n"
    "s_cmp_lt_u32 s54, 28\n"
    "s_cselect_b32 s54, s54, -1\n"
    "s_mov_b32 s11, 0xffffff00\n"
    "s_lshl_b32 s98, s48, 12\n"
    "v_mov_b32 v17, 0x7f000000\n"
    "v_cmp_gt_u32 vcc, 16, v0\n"
    "v_lshlrev_b32 v7, 5, v18\n"
    "v_lshrrev_b32 v19, 4, v1\n"
    "v_bfe_u32 v14, v1, 3, 1\n"
    "v_cndmask_b32 v9, v17, v9, vcc\n"
    "v_lshl_add_u32 v14, v19, 3, v14\n"
    "s_lshl_b32 s58, s48, 6\n"
    "v_add_u32_e64 v14, s58, v14\n"
    "v_mul_u32_u24 v14, 3072, v14\n"
    "v_and_b32 v19, 1, v19\n"
    "v_lshlrev_b32 v19, 1, v19\n"
    "v_xor_b32 v18, v18, v19\n"
    "s_lshl_b32 s58, s50, 7\n"
    "v_lshl_add_u32 v18, v18, 4, s58\n"
    "v_add_u32_e64 v3, v18, v14\n"
    "v_add_u32 v4, 98304, v3\n"
    "v_xor_b32 v5, 64, v3\n"
    "v_xor_b32 v6, 64, v4\n"
    "s_bfe_u32 s99, s49, 0x20001\n"
    "s_mul_i32 s0, s51, 786432\n"
    "s_add_u32 s58, s98, 81920\n"
    "v_add_u32_e64 v10, s58, v13\n"
    "v_add_u32 v22, 65536, v10\n"
    "s_and_b32 s58, s50, 2\n"
    "s_lshr_b32 s59, s50, 2\n"
    "s_add_u32 s58, s58, s59\n"
    "s_lshl_b32 s58, s58, 2\n"
    "v_xor_b32_e64 v14, s58, v2\n"
    "v_mul_u32_u24 v14, 272, v14\n"
    "s_lshl_b32 s58, s48, 6\n"
    "v_lshl_add_u32 v105, v12, 4, s58\n"
    "v_add_u32 v105, v105, v14\n"
    "v_bfe_u32 v66, v0, 1, 5\n"
    "v_lshlrev_b32 v66, 4, v66\n"
    "v_and_b32 v67, 1, v0\n"
    "v_lshl_add_u32 v66, v67, 9, v66\n"
    "s_xor_b32 s58, s48, s51\n"
    "s_lshl_b32 s58, s58, 10\n"
    "v_add_u32 v66, s58, v66\n"
    "v_lshrrev_b32 v67, 7, v0\n"
    "v_cvt_f32_u32 v76, v67\n"
    "v_lshlrev_b32 v65, 4, v0\n"
    "s_lshl3_add_u32 s67, s49, s50\n"
    "s_lshl_b32 s67, s67, 7\n"
    "s_add_u32 s67, s67, 458752\n"
    "s_lshr_b32 s51, s57, 2\n"
    "s_lshl3_add_u32 s50, s56, s51\n"
    "s_and_b32 s53, s57, 3\n"
    "s_lshl_b32 s58, s52, 15\n"
    "s_lshl2_add_u32 s59, s53, s48\n"
    "s_lshl_b32 s59, s59, 6\n"
    "s_add_u32 s58, s58, s59\n"
    "v_lshl_add_u32 v16, v2, 10, s58\n"
    "v_lshl_add_u32 v11, v12, 4, v16\n"
    "v_lshrrev_b32 v14, 4, v1\n"
    "v_and_b32 v16, 0xff, v13\n"
    "v_lshl_add_u32 v16, v14, 12, v16\n"
    "s_lshl_b32 s60, s53, 8\n"
    "v_add_u32 v16, s60, v16\n"
    "v_xor_b32_e64 v17, 64, v16\n"
    "v_xor_b32 v18, 128, v16\n"
    "v_xor_b32 v19, 192, v16\n"
    "s_lshl2_add_u32 s10, s51, s48\n"
    "s_lshl_b32 s10, s10, 14\n"
    "s_lshl2_add_u32 s60, s48, s53\n"
    "s_lshl_b32 s60, s60, 9\n"
    "s_add_u32 s60, s60, 20480\n"
    "v_mul_u32_u24 v50, 24576, v12\n"
    "v_lshl_add_u32 v14, v2, 4, s60\n"
    "v_add_u32 v50, v50, v14\n"
    "v_lshlrev_b32_e64 v49, 2, v1\n"
    "s_movk_i32 s9, 18432\n"
    "s_lshl2_add_u32 s63, s50, s53\n"
    "s_lshl_b32 s63, s63, 9\n"
    "s_add_u32 s63, s63, 327680\n"
    "s_cmp_eq_u32 s48, 0\n"
    "s_cselect_b32 s63, s63, 0x7f000000\n"
    "v_lshl_add_u32 v106, v2, 4, s63\n"
    "s_lshl_b32 s58, s50, 4\n"
    "s_add_u32 s58, s58, 524288\n"
    "s_lshl2_add_u32 s59, s53, s48\n"
    "s_cmp_eq_u32 s59, 4\n"
    "s_cselect_b32 s58, s58, 0x7f000000\n"
    "v_lshl_add_u32 v107, v1, 13, s58\n"
    "v_lshlrev_b32_e64 v108, 3, v2\n"
    "v_bfe_u32 v14, v1, 2, 2\n"
    "v_xor_b32 v42, s48, v14\n"
    "v_lshlrev_b32 v42, 6, v42\n"
    "v_lshl_add_u32 v42, v14, 10, v42\n"
    "v_lshl_add_u32 v42, v12, 8, v42\n"
    "v_bfe_u32 v14, v1, 4, 1\n"
    "v_lshl_add_u32 v42, v14, 5, v42\n"
    "v_and_b32_e64 v14, 3, v1\n"
    "v_lshl_add_u32 v42, v14, 3, v42\n"
    "v_add_u32 v102, 146944, v42\n"
    "v_add_u32 v101, 81408, v42\n"
    "v_add_u32 v100, 15872, v42\n"
    "s_lshl_b32 s58, s48, 7\n"
    "v_lshl_add_u32 v103, v12, 4, s58\n"
    "s_lshr_b32 s58, s57, 3\n"
    "s_lshl3_add_u32 s58, s58, s56\n"
    "s_and_b32 s59, s57, 7\n"
    "s_cselect_b32 s58, s58, 99\n"
    "s_sub_u32 s69, s59, 1\n"
    "s_cmp_eq_u32 s48, 3\n"
    "s_cselect_b32 s69, s69, s48\n"
    "s_cselect_b32 s59, 0, s59\n"
    "v_lshl_add_u32 v14, v1, 3, s59\n"
    "s_cmp_eq_u32 s69, 0\n"
    "s_cselect_b64 s[40:41], s[36:37], s[40:41]\n"
    "s_cmp_eq_u32 s69, 2\n"
    "s_cselect_b64 s[40:41], s[44:45], s[40:41]\n"
    "s_cselect_b32 s59, 16, 7\n"
    "s_cselect_b32 s60, 1, 28\n"
    "s_lshl_b32 s59, s58, s59\n"
    "s_cmp_lt_u32 s58, 28\n"
    "s_cselect_b32 s42, s38, 0\n"
    "s_cmp_gt_u32 s69, 2\n"
    "s_cselect_b32 s42, 0, s42\n"
    "v_mul_u32_u24 v14, s60, v14\n"
    "v_lshl_add_u32 v64, v14, 7, s59\n"
    "v_mov_b32_e64 v14, 0\n"
    "ds_write_b32 v49, v14 offset:13248\n"
    "s_waitcnt lgkmcnt(0)\n"
    "s_barrier\n"
    "s_waitcnt vmcnt(5)\n"
    "v_lshlrev_b32 v24, 16, v62\n"
    "v_and_b32 v25, 0xffff0000, v62\n"
    "v_lshlrev_b32_e64 v26, 16, v63\n"
    "v_and_b32 v27, 0xffff0000, v63\n"
    "v_mul_f32 v28, 0xbfb8aa3b, v24\n"
    "v_mul_f32 v29, 0xbfb8aa3b, v25\n"
    "v_mul_f32 v30, 0xbfb8aa3b, v26\n"
    "v_mul_f32 v31, 0xbfb8aa3b, v27\n"
    "v_exp_f32 v28, v28\n"
    "v_exp_f32 v29, v29\n"
    "v_exp_f32 v30, v30\n"
    "v_exp_f32 v31, v31\n"
    "v_add_f32 v28, 1.0, v28\n"
    "v_add_f32 v29, 1.0, v29\n"
    "v_add_f32 v30, 1.0, v30\n"
    "v_add_f32 v31, 1.0, v31\n"
    "v_rcp_f32 v28, v28\n"
    "v_rcp_f32 v29, v29\n"
    "v_rcp_f32 v30, v30\n"
    "v_rcp_f32 v31, v31\n"
    "v_mul_f32 v28, v28, v24\n"
    "v_mul_f32 v29, v29, v25\n"
    "v_mul_f32 v30, v30, v26\n"
    "v_mul_f32 v31, v31, v27\n"
    "v_cvt_pk_bf16_f32 v24, v28, 0\n"
    "v_cvt_pk_bf16_f32 v25, v29, 0\n"
    "v_cvt_pk_bf16_f32 v26, v30, 0\n"
    "v_cvt_pk_bf16_f32 v27, v31, 0\n"
    "v_lshlrev_b32 v28, 16, v24\n"
    "v_lshlrev_b32 v29, 16, v25\n"
    "v_lshlrev_b32 v30, 16, v26\n"
    "v_lshlrev_b32 v31, 16, v27\n"
    "v_lshrrev_b32 v40, 4, v0\n"
    "v_and_b32 v41, 3, v40\n"
    "v_lshrrev_b32_e64 v40, 2, v40\n"
    "v_lshl_add_u32 v40, v40, 8, v7\n"
    "v_lshl_add_u32 v40, v41, 3, v40\n"
    "v_and_b32 v41, 0xff, v13\n"
    "s_waitcnt vmcnt(4)\n"
    "v_readfirstlane_b32 s55, v69\n"
    "s_nop 0\n"
    "s_add_u32 s55, s55, 1\n"
    "s_mul_i32 s8, s55, 0x9e3779b1\n"
    "v_bfe_u32 v52, s55, 0, 8\n"
    "v_bfe_u32 v53, s55, 8, 8\n"
    "v_bfe_u32 v54, s55, 16, 8\n"
    "v_bfe_u32 v55, s55, 24, 8\n"
    "s_cmp_lt_u32 s2, 64\n"
    "s_cbranch_scc0 .Lk_Lnounit\n"
    "s_waitcnt vmcnt(3)\n"
    "s_nop 0\n"
    "v_dot2_f32_bf16 v32, v112, v24, 0\n"
    "v_dot2_f32_bf16 v33, v112, v28, 0\n"
    "v_dot2_f32_bf16 v34, v113, v24, 0\n"
    "v_dot2_f32_bf16 v35, v113, v28, 0\n"
    "v_dot2_f32_bf16 v36, v114, v24, 0\n"
    "v_dot2_f32_bf16 v37, v114, v28, 0\n"
    "v_dot2_f32_bf16 v38, v115, v24, 0\n"
    "v_dot2_f32_bf16 v39, v115, v28, 0\n"
    "s_waitcnt vmcnt(2)\n"
    "v_dot2c_f32_bf16 v32, v116, v25\n"
    "v_dot2c_f32_bf16 v33, v116, v29\n"
    "v_dot2c_f32_bf16 v34, v117, v25\n"
    "v_dot2c_f32_bf16 v35, v117, v29\n"
    "v_dot2c_f32_bf16 v36, v118, v25\n"
    "v_dot2c_f32_bf16 v37, v118, v29\n"
    "v_dot2c_f32_bf16 v38, v119, v25\n"
    "v_dot2c_f32_bf16 v39, v119, v29\n"
    "s_waitcnt vmcnt(1)\n"
    "v_dot2c_f32_bf16 v32, v120, v26\n"
    "v_dot2c_f32_bf16 v33, v120, v30\n"
    "v_dot2c_f32_bf16 v34, v121, v26\n"
    "v_dot2c_f32_bf16 v35, v121, v30\n"
    "v_dot2c_f32_bf16 v36, v122, v26\n"
    "v_dot2c_f32_bf16 v37, v122, v30\n"
    "v_dot2c_f32_bf16 v38, v123, v26\n"
    "v_dot2c_f32_bf16 v39, v123, v30\n"
    "s_waitcnt vmcnt(0)\n"
    "v_dot2c_f32_bf16 v32, v124, v27\n"
    "v_dot2c_f32_bf16 v33, v124, v31\n"
    "v_dot2c_f32_bf16 v34, v125, v27\n"
    "v_dot2c_f32_bf16 v35, v125, v31\n"
    "v_dot2c_f32_bf16 v36, v126, v27\n"
    "v_dot2c_f32_bf16 v37, v126, v31\n"
    "v_dot2c_f32_bf16 v38, v127, v27\n"
    "v_dot2c_f32_bf16 v39, v127, v31\n"
    "v_permlane32_swap_b32 v32, v36\n"
    "v_permlane32_swap_b32 v33, v37\n"
    "v_permlane32_swap_b32 v34, v38\n"
    "v_permlane32_swap_b32_e64 v35, v39\n"
    "v_pk_add_f32 v[32:33], v[32:33], v[36:37]\n"
    "v_pk_add_f32 v[34:35], v[34:35], v[38:39]\n"
    "s_nop 1\n"
    "v_permlane16_swap_b32 v32, v34\n"
    "v_permlane16_swap_b32_e64 v33, v35\n"
    "v_pk_add_f32 v[32:33], v[32:33], v[34:35]\n"
    "s_nop 1\n"
    "v_add_f32_dpp v32, v32, v32 row_ror:8 row_mask:0xf bank_mask:0xf\n"
    "v_add_f32_dpp v33, v33, v33 row_ror:8 row_mask:0xf bank_mask:0xf\n"
    "ds_write_b64 v40, v[32:33] offset:4096\n"
    "v_mov_b32_e64 v14, 1\n"
    "s_mov_b64 exec, 1\n"
    "ds_add_u32 v14, v14 offset:13247\n"
    "s_mov_b64 exec, -1\n"
    "s_waitcnt lgkmcnt(0)\n"
    "s_mov_b32 s1, 8388608\n"
    "v_mov_b32_e64 v14, 0\n"
    ".Lk_Lw8_0:\n"
    "ds_read_b32 v44, v14 offset:13248\n"
    "ds_read_b128 v[112:115], v41 offset:4096\n"
    "ds_read_b128 v[116:119], v41 offset:4352\n"
    "ds_read_b128 v[120:123], v41 offset:4608\n"
    "ds_read_b128 v[124:127], v41 offset:4864\n"
    "s_sub_u32 s1, s1, 1\n"
    "s_cbranch_scc1 .Lk_Lx8_0\n"
    "s_waitcnt lgkmcnt(4)\n"
    "v_readfirstlane_b32 s60, v44\n"
    "s_waitcnt lgkmcnt(0)\n"
    "s_cmp_lt_u32 s60, 4\n"
    "s_cbranch_scc1 .Lk_Lw8_0\n"
    ".Lk_Lx8_0:\n"
    "s_waitcnt lgkmcnt(0)\n"
    "v_pk_add_f32 v[112:113], v[112:113], v[116:117]\n"
    "v_pk_add_f32 v[114:115], v[114:115], v[118:119]\n"
    "v_pk_add_f32 v[120:121], v[120:121], v[124:125]\n"
    "v_pk_add_f32 v[122:123], v[122:123], v[126:127]\n"
    "v_pk_add_f32 v[112:113], v[112:113], v[120:121]\n"
    "v_pk_add_f32 v[114:115], v[114:115], v[122:123]\n"
    "v_and_or_b32 v112, v112, s11, v52\n"
    "v_and_or_b32 v113, v113, s11, v53\n"
    "v_and_or_b32 v114, v114, s11, v54\n"
    "v_and_or_b32 v115, v115, s11, v55\n"
    "buffer_store_dwordx4 v[112:115], v9, s[20:23], 0 offen sc1\n"
    "v_mov_b32 v8, 0x7f000000\n"
    "v_mov_b32 v9, 0x7f000000\n"
    ".Lk_Lnounit:\n"
    "s_mov_b32 s1, 8388608\n"
    "v_mov_b32 v14, 0\n"
    ".Lk_Lw8_2:\n"
    "s_sleep 1\n"
    "ds_read_b32 v44, v14 offset:13256\n"
    "s_sub_u32 s1, s1, 1\n"
    "s_cbranch_scc1 .Lk_Lx8_2\n"
    "s_waitcnt lgkmcnt(0)\n"
    "v_readfirstlane_b32 s60, v44\n"
    "s_waitcnt lgkmcnt(0)\n"
    "s_cmp_lt_u32 s60, 4\n"
    "s_cbranch_scc1 .Lk_Lw8_2\n"
    ".Lk_Lx8_2:\n"
    "s_waitcnt lgkmcnt(0)\n"
    "s_mul_i32 s3, s99, 6144\n"
    "s_add_u32 s3, s3, s0\n"
    "s_lshl_b32 s61, s99, 14\n"
    "s_add_u32 s61, s61, s98\n"
    "s_add_u32 s99, s99, 1\n"
    "s_and_b32 s99, s99, 3\n"
    "s_add_u32 m0, s61, 80896\n"
    "s_nop 0\n"
    "buffer_load_dwordx4 v3, s[16:19], s3 offen offset:1024 lds\n"
    "buffer_load_dwordx4 v5, s[16:19], s3 offen offset:2048 lds\n"
    "s_add_u32 m0, s61, 82944\n"
    "s_nop 0\n"
    "buffer_load_dwordx4 v4, s[16:19], s3 offen offset:1024 lds\n"
    "buffer_load_dwordx4 v6, s[16:19], s3 offen offset:2048 lds\n"
    "s_mul_i32 s3, s99, 6144\n"
    "s_add_u32 s3, s3, s0\n"
    "s_lshl_b32 s61, s99, 14\n"
    "s_add_u32 s61, s61, s98\n"
    "s_add_u32 s99, s99, 1\n"
    "s_and_b32 s99, s99, 3\n"
    "s_add_u32 m0, s61, 80896\n"
    "s_nop 0\n"
    "buffer_load_dwordx4 v3, s[16:19], s3 offen offset:1024 lds\n"
    "buffer_load_dwordx4 v5, s[16:19], s3 offen offset:2048 lds\n"
    "s_add_u32 m0, s61, 82944\n"
    "s_nop 0\n"
    "buffer_load_dwordx4 v4, s[16:19], s3 offen offset:1024 lds\n"
    "buffer_load_dwordx4 v6, s[16:19], s3 offen offset:2048 lds\n"
    "s_mul_i32 s3, s99, 6144\n"
    "s_add_u32 s3, s3, s0\n"
    "s_lshl_b32 s61, s99, 14\n"
    "s_add_u32 s61, s61, s98\n"
    "s_add_u32 s99, s99, 1\n"
    "s_and_b32 s99, s99, 3\n"
    "s_add_u32 m0, s61, 80896\n"
    "s_nop 0\n"
    "buffer_load_dwordx4 v3, s[16:19], s3 offen offset:1024 lds\n"
    "buffer_load_dwordx4 v5, s[16:19], s3 offen offset:2048 lds\n"
    "s_add_u32 m0, s61, 82944\n"
    "s_nop 0\n"
    "buffer_load_dwordx4 v4, s[16:19], s3 offen offset:1024 lds\n"
    "buffer_load_dwordx4 v6, s[16:19], s3 offen offset:2048 lds\n"
    "s_mul_i32 s3, s99, 6144\n"
    "s_add_u32 s3, s3, s0\n"
    "s_lshl_b32 s61, s99, 14\n"
    "s_add_u32 s61, s61, s98\n"
    "s_add_u32 s99, s99, 1\n"
    "s_and_b32 s99, s99, 3\n"
    "s_add_u32 m0, s61, 80896\n"
    "s_nop 0\n"
    "buffer_load_dwordx4 v3, s[16:19], s3 offen offset:1024 lds\n"
    "buffer_load_dwordx4 v5, s[16:19], s3 offen offset:2048 lds\n"
    "s_add_u32 m0, s61, 82944\n"
    "s_nop 0\n"
    "buffer_load_dwordx4 v4, s[16:19], s3 offen offset:1024 lds\n"
    "buffer_load_dwordx4 v6, s[16:19], s3 offen offset:2048 lds\n"
    "s_mov_b32 s60, 16384\n"
    "buffer_load_dwordx4 v[80:83], v65, s[20:23], s60 offen sc1\n"
    "s_mov_b32 s60, 28672\n"
    "buffer_load_dwordx4 v[84:87], v65, s[20:23], s60 offen sc1\n"
    "s_mov_b32 s60, 40960\n"
    "buffer_load_dwordx4 v[88:91], v65, s[20:23], s60 offen sc1\n"
    "s_mov_b32 s60, 53248\n"
    "buffer_load_dwordx4 v[92:95], v65, s[20:23], s60 offen sc1\n"
    "s_sub_u32 s3, 786432, s0\n"
    "s_add_u32 m0, s98, 146432\n"
    "s_nop 0\n"
    "buffer_load_dwordx4 v3, s[16:19], s3 offen offset:1024 lds\n"
    "buffer_load_dwordx4 v5, s[16:19], s3 offen offset:2048 lds\n"
    "s_add_u32 m0, s98, 148480\n"
    "s_nop 0\n"
    "buffer_load_dwordx4 v4, s[16:19], s3 offen offset:1024 lds\n"
    "buffer_load_dwordx4 v6, s[16:19], s3 offen offset:2048 lds\n"
    "s_waitcnt vmcnt(7)\n"
    "v_xor_b32 v80, v52, v80\n"
    "v_xor_b32 v81, v53, v81\n"
    "v_xor_b32 v82, v54, v82\n"
    "v_xor_b32 v83, v55, v83\n"
    "v_or_b32 v14, v80, v81\n"
    "v_or3_b32 v14, v14, v82, v83\n"
    "s_waitcnt vmcnt(6)\n"
    "v_xor_b32 v84, v52, v84\n"
    "v_xor_b32 v85, v53, v85\n"
    "v_xor_b32 v86, v54, v86\n"
    "v_xor_b32_e64 v87, v55, v87\n"
    "v_or3_b32 v14, v14, v84, v85\n"
    "v_or3_b32 v14, v14, v86, v87\n"
    "v_pk_add_f32 v[70:71], v[80:81], v[84:85]\n"
    "v_pk_add_f32 v[72:73], v[82:83], v[86:87]\n"
    "s_waitcnt vmcnt(5)\n"
    "v_xor_b32 v88, v52, v88\n"
    "v_xor_b32 v89, v53, v89\n"
    "v_xor_b32 v90, v54, v90\n"
    "v_xor_b32_e64 v91, v55, v91\n"
    "v_or3_b32 v14, v14, v88, v89\n"
    "v_or3_b32 v14, v14, v90, v91\n"
    "v_pk_add_f32 v[70:71], v[70:71], v[88:89]\n"
    "v_pk_add_f32 v[72:73], v[72:73], v[90:91]\n"
    "s_waitcnt vmcnt(4)\n"
    "v_xor_b32 v92, v52, v92\n"
    "v_xor_b32 v93, v53, v93\n"
    "v_xor_b32 v94, v54, v94\n"
    "v_xor_b32_e64 v95, v55, v95\n"
    "v_or3_b32 v14, v14, v92, v93\n"
    "v_or3_b32 v14, v14, v94, v95\n"
    "v_pk_add_f32 v[70:71], v[70:71], v[92:93]\n"
    "v_pk_add_f32 v[72:73], v[72:73], v[94:95]\n"
    "v_and_b32 v14, 0xff, v14\n"
    "v_cmp_ne_u32 vcc, 0, v14\n"
    "s_nop 0\n"
    "v_cvt_pk_bf16_f32 v70, 0, v70\n"
    "v_cvt_pk_bf16_f32 v71, 0, v71\n"
    "v_cvt_pk_bf16_f32 v72, 0, v72\n"
    "v_cvt_pk_bf16_f32 v73, 0, v73\n"
    "v_pk_add_f32 v[70:71], v[70:71], v[76:77] op_sel_hi:[1,0]\n"
    "v_pk_add_f32 v[72:73], v[72:73], v[76:77] op_sel_hi:[1,0]\n"
    "v_cvt_pk_bf16_f32 v70, 0, v70\n"
    "v_cvt_pk_bf16_f32 v71, 0, v71\n"
    "v_cvt_pk_bf16_f32 v72, 0, v72\n"
    "v_cvt_pk_bf16_f32 v73, 0, v73\n"
    "ds_write_b128 v66, v[70:73]\n"
    "s_cbranch_vccnz .Lk_R_mod\n"
    ".Lk_B_mod:\n"
    "s_setprio 0\n"
    "s_waitcnt lgkmcnt(0)\n"
    "s_barrier\n"
    "buffer_load_dwordx4 v[112:115], v8, s[4:7], 0 offen\n"
    "buffer_load_dwordx4 v[116:119], v8, s[4:7], s64 offen\n"
    "buffer_load_dwordx4 v[120:123], v8, s[4:7], s65 offen\n"
    "buffer_load_dwordx4 v[124:127], v8, s[4:7], s9 offen\n"
    "s_sub_u32 s3, 786432, s0\n"
    "buffer_load_dwordx4 v[208:211], v3, s[16:19], s3 offen offset:0\n"
    "buffer_load_dwordx4 v[216:219], v4, s[16:19], s3 offen offset:0\n"
    "s_sub_u32 s3, 792576, s0\n"
    "buffer_load_dwordx4 v[212:215], v5, s[16:19], s3 offen offset:0\n"
    "buffer_load_dwordx4 v[220:223], v6, s[16:19], s3 offen offset:0\n"
    "s_sub_u32 s3, 798720, s0\n"
    "buffer_load_dwordx4 v[224:227], v3, s[16:19], s3 offen offset:0\n"
    "buffer_load_dwordx4 v[232:235], v4, s[16:19], s3 offen offset:0\n"
    "s_sub_u32 s3, 804864, s0\n"
    "buffer_load_dwordx4 v[228:231], v5, s[16:19], s3 offen offset:0\n"
    "buffer_load_dwordx4 v[236:239], v6, s[16:19], s3 offen offset:0\n"
    "s_waitcnt vmcnt(12)\n"
    "s_barrier\n"
    "s_barrier\n"
    "s_sub_u32 s3, 792576, s0\n"
    "s_add_u32 m0, s98, 80896\n"
    "s_nop 0\n"
    "buffer_load_dwordx4 v3, s[16:19], s3 offen offset:1024 lds\n"
    "buffer_load_dwordx4 v5, s[16:19], s3 offen offset:2048 lds\n"
    "s_add_u32 m0, s98, 82944\n"
    "s_nop 0\n"
    "buffer_load_dwordx4 v4, s[16:19], s3 offen offset:1024 lds\n"
    "buffer_load_dwordx4 v6, s[16:19], s3 offen offset:2048 lds\n"
    "s_sub_u32 s3, 798720, s0\n"
    "s_add_u32 m0, s98, 97280\n"
    "s_nop 0\n"
    "buffer_load_dwordx4 v3, s[16:19], s3 offen offset:1024 lds\n"
    "buffer_load_dwordx4 v5, s[16:19], s3 offen offset:2048 lds\n"
    "s_add_u32 m0, s98, 99328\n"
    "s_nop 0\n"
    "buffer_load_dwordx4 v4, s[16:19], s3 offen offset:1024 lds\n"
    "buffer_load_dwordx4 v6, s[16:19], s3 offen offset:2048 lds\n"
    "s_sub_u32 s3, 804864, s0\n"
    "s_add_u32 m0, s98, 113664\n"
    "s_nop 0\n"
    "buffer_load_dwordx4 v3, s[16:19], s3 offen offset:1024 lds\n"
    "buffer_load_dwordx4 v5, s[16:19], s3 offen offset:2048 lds\n"
    "s_add_u32 m0, s98, 115712\n"
    "s_nop 0\n"
    "buffer_load_dwordx4 v4, s[16:19], s3 offen offset:1024 lds\n"
    "buffer_load_dwordx4 v6, s[16:19], s3 offen offset:2048 lds\n"
    "s_waitcnt vmcnt(8)\n"
    "s_barrier\n"
    "s_add_u32 s3, s0, 0\n"
    "s_add_u32 m0, s98, 131072\n"
    "s_nop 0\n"
    "buffer_load_dwordx4 v3, s[16:19], s3 offen lds\n"
    "s_add_u32 m0, s98, 133120\n"
    "s_nop 0\n"
    "buffer_load_dwordx4 v4, s[16:19], s3 offen lds\n"
    "s_add_u32 s3, s0, 6144\n"
    "s_add_u32 m0, s98, 132096\n"
    "s_nop 0\n"
    "buffer_load_dwordx4 v5, s[16:19], s3 offen lds\n"
    "s_add_u32 m0, s98, 134144\n"
    "s_nop 0\n"
    "buffer_load_dwordx4 v6, s[16:19], s3 offen lds\n"
    "s_waitcnt vmcnt(8)\n"
    "s_barrier\n"
    "s_add_u32 s3, s0, 12288\n"
    "s_add_u32 m0, s98, 147456\n"
    "s_nop 0\n"
    "buffer_load_dwordx4 v3, s[16:19], s3 offen lds\n"
    "s_add_u32 m0, s98, 149504\n"
    "s_nop 0\n"
    "buffer_load_dwordx4 v4, s[16:19], s3 offen lds\n"
    "s_add_u32 s3, s0, 18432\n"
    "s_add_u32 m0, s98, 148480\n"
    "s_nop 0\n"
    "buffer_load_dwordx4 v5, s[16:19], s3 offen lds\n"
    "s_add_u32 m0, s98, 150528\n"
    "s_nop 0\n"
    "buffer_load_dwordx4 v6, s[16:19], s3 offen lds\n"
    "s_waitcnt vmcnt(8)\n"
    "s_barrier\n"
    "s_barrier\n"
    "s_nop 0\n"
    "ds_write_b128 v10, v[208:211] offset:0\n"
    "ds_write_b128 v10, v[212:215] offset:1024\n"
    "ds_write_b128 v10, v[216:219] offset:2048\n"
    "ds_write_b128 v10, v[220:223] offset:3072\n"
    "ds_write_b128 v10, v[224:227] offset:16384\n"
    "ds_write_b128 v10, v[228:231] offset:17408\n"
    "ds_write_b128 v10, v[232:235] offset:18432\n"
    "ds_write_b128 v10, v[236:239] offset:19456\n"
    "v_dot2_f32_bf16 v32, v112, v24, 0\n"
    "v_dot2_f32_bf16 v33, v112, v28, 0\n"
    "v_dot2_f32_bf16 v34, v113, v24, 0\n"
    "v_dot2_f32_bf16 v35, v113, v28, 0\n"
    "v_dot2_f32_bf16 v36, v114, v24, 0\n"
    "v_dot2_f32_bf16 v37, v114, v28, 0\n"
    "v_dot2_f32_bf16 v38, v115, v24, 0\n"
    "v_dot2_f32_bf16 v39, v115, v28, 0\n"
    "v_dot2c_f32_bf16 v32, v116, v25\n"
    "v_dot2c_f32_bf16 v33, v116, v29\n"
    "v_dot2c_f32_bf16 v34, v117, v25\n"
    "v_dot2c_f32_bf16 v35, v117, v29\n"
    "v_dot2c_f32_bf16 v36, v118, v25\n"
    "v_dot2c_f32_bf16 v37, v118, v29\n"
    "v_dot2c_f32_bf16 v38, v119, v25\n"
    "v_dot2c_f32_bf16 v39, v119, v29\n"
    "v_dot2c_f32_bf16 v32, v120, v26\n"
    "v_dot2c_f32_bf16 v33, v120, v30\n"
    "v_dot2c_f32_bf16 v34, v121, v26\n"
    "v_dot2c_f32_bf16 v35, v121, v30\n"
    "v_dot2c_f32_bf16 v36, v122, v26\n"
    "v_dot2c_f32_bf16 v37, v122, v30\n"
    "v_dot2c_f32_bf16 v38, v123, v26\n"
    "v_dot2c_f32_bf16 v39, v123, v30\n"
    "v_dot2c_f32_bf16 v32, v124, v27\n"
    "v_dot2c_f32_bf16 v33, v124, v31\n"
    "v_dot2c_f32_bf16 v34, v125, v27\n"
    "v_dot2c_f32_bf16 v35, v125, v31\n"
    "v_dot2c_f32_bf16 v36, v126, v27\n"
    "v_dot2c_f32_bf16 v37, v126, v31\n"
    "v_dot2c_f32_bf16 v38, v127, v27\n"
    "v_dot2c_f32_bf16 v39, v127, v31\n"
    "v_permlane32_swap_b32 v32, v36\n"
    "v_permlane32_swap_b32 v33, v37\n"
    "v_permlane32_swap_b32 v34, v38\n"
    "v_permlane32_swap_b32 v35, v39\n"
    "v_pk_add_f32 v[32:33], v[32:33], v[36:37]\n"
    "v_pk_add_f32 v[34:35], v[34:35], v[38:39]\n"
    "s_nop 1\n"
    "v_permlane16_swap_b32 v32, v34\n"
    "v_permlane16_swap_b32_e64 v33, v35\n"
    "v_pk_add_f32 v[32:33], v[32:33], v[34:35]\n"
    "s_nop 1\n"
    "v_add_f32_dpp v32, v32, v32 row_ror:8 row_mask:0xf bank_mask:0xf\n"
    "v_add_f32_dpp v33, v33, v33 row_ror:8 row_mask:0xf bank_mask:0xf\n"
    "ds_write_b64 v40, v[32:33] offset:4096\n"
    "s_waitcnt vmcnt(0) lgkmcnt(0)\n"
    "s_barrier\n"
    "s_cmp_eq_u32 s69, 2\n"
    "s_mov_b32 s58, 0xffffffff\n"
    "s_cselect_b32 vcc_lo, 0x0, s58\n"
    "s_mov_b32 s58, 0xffff\n"
    "s_cselect_b32 vcc_hi, 0x0, s58\n"
    "v_mov_b32 v14, 0x7f000000\n"
    "v_cndmask_b32 v14, v14, v64, vcc\n"
    "buffer_load_dword v109, v14, s[40:43], 0 offen\n"
    "v_not_b32 v84, s55\n"
    "v_not_b32 v90, s8\n"
    "buffer_store_dwordx4 v[84:87], v106, s[20:23], 0 offen sc1\n"
    "v_mov_b64 v[88:89], 0\n"
    "s_mov_b32 exec_hi, 15\n"
    "buffer_store_dwordx3 v[88:90], v107, s[20:23], 0 offen sc1\n"
    "s_mov_b64 exec, -1\n"
    "ds_read_b128 v[112:115], v41 offset:4096\n"
    "ds_read_b128 v[116:119], v41 offset:4352\n"
    "ds_read_b128 v[120:123], v41 offset:4608\n"
    "ds_read_b128 v[124:127], v41 offset:4864\n"
    "s_waitcnt lgkmcnt(2)\n"
    "v_add_f32 v112, v112, v116\n"
    "v_add_f32 v113, v113, v117\n"
    "v_add_f32 v114, v114, v118\n"
    "v_add_f32 v115, v115, v119\n"
    "s_waitcnt lgkmcnt(0)\n"
    "v_add_f32 v120, v120, v124\n"
    "v_add_f32 v121, v121, v125\n"
    "v_add_f32 v122, v122, v126\n"
    "v_add_f32 v123, v123, v127\n"
    "v_add_f32 v112, v112, v120\n"
    "v_add_f32 v113, v113, v121\n"
    "v_add_f32 v114, v114, v122\n"
    "v_add_f32_e64 v115, v115, v123\n"
    "v_and_or_b32 v112, v112, s11, v52\n"
    "v_and_or_b32 v113, v113, s11, v53\n"
    "v_and_or_b32 v114, v114, s11, v54\n"
    "v_and_or_b32 v115, v115, s11, v55\n"
    "buffer_store_dwordx4 v[112:115], v9, s[20:23], 0 offen sc1\n"
    "s_setprio 2\n"
    "s_mov_b32 s1, 8388608\n"
    "v_mov_b32_e64 v14, 0\n"
    ".Lk_Lw8_3:\n"
    "s_sleep 2\n"
    "ds_read_b32 v44, v14 offset:13260\n"
    "s_sub_u32 s1, s1, 1\n"
    "s_cbranch_scc1 .Lk_Lx8_3\n"
    "s_waitcnt lgkmcnt(0)\n"
    "v_readfirstlane_b32 s60, v44\n"
    "s_waitcnt lgkmcnt(0)\n"
    "s_cmp_lt_u32 s60, 4\n"
    "s_cbranch_scc1 .Lk_Lw8_3\n"
    ".Lk_Lx8_3:\n"
    "s_waitcnt lgkmcnt(0)\n"
    "s_add_u32 m0, s98, 114688\n"
    "s_and_b32 s3, s10, 0x7ffff\n"
    "s_add_u32 s10, s10, 65536\n"
    "buffer_load_dwordx4 v16, s[24:27], s3 offen offset:0 lds\n"
    "buffer_load_dwordx4 v17, s[24:27], s3 offen offset:1024 lds\n"
    "buffer_load_dwordx4 v18, s[24:27], s3 offen offset:2048 lds\n"
    "buffer_load_dwordx4 v19, s[24:27], s3 offen offset:3072 lds\n"
    "s_mov_b32 s1, 8388608\n"
    "v_mov_b32 v14, 0\n"
    ".Lk_Lw8_5:\n"
    "s_sleep 2\n"
    "ds_read_b32 v44, v14 offset:13268\n"
    "s_sub_u32 s1, s1, 1\n"
    "s_cbranch_scc1 .Lk_Lx8_5\n"
    "s_waitcnt lgkmcnt(0)\n"
    "v_readfirstlane_b32 s60, v44\n"
    "s_waitcnt lgkmcnt(0)\n"
    "s_cmp_lt_u32 s60, 4\n"
    "s_cbranch_scc1 .Lk_Lw8_5\n"
    ".Lk_Lx8_5:\n"
    "s_waitcnt lgkmcnt(0)\n"
    "s_add_u32 m0, s98, 131072\n"
    "s_and_b32 s3, s10, 0x7ffff\n"
    "s_add_u32 s10, s10, 65536\n"
    "buffer_load_dwordx4 v16, s[24:27], s3 offen offset:0 lds\n"
    "buffer_load_dwordx4 v17, s[24:27], s3 offen offset:1024 lds\n"
    "buffer_load_dwordx4 v18, s[24:27], s3 offen offset:2048 lds\n"
    "buffer_load_dwordx4 v19, s[24:27], s3 offen offset:3072 lds\n"
    "s_add_u32 m0, s98, 147456\n"
    "s_and_b32 s3, s10, 0x7ffff\n"
    "s_add_u32 s10, s10, 65536\n"
    "buffer_load_dwordx4 v16, s[24:27], s3 offen offset:0 lds\n"
    "buffer_load_dwordx4 v17, s[24:27], s3 offen offset:1024 lds\n"
    "buffer_load_dwordx4 v18, s[24:27], s3 offen offset:2048 lds\n"
    "buffer_load_dwordx4 v19, s[24:27], s3 offen offset:3072 lds\n"
    "s_add_u32 m0, s98, 15872\n"
    "s_and_b32 s3, s10, 0x7ffff\n"
    "s_add_u32 s10, s10, 65536\n"
    "buffer_load_dwordx4 v16, s[24:27], s3 offen offset:0 lds\n"
    "buffer_load_dwordx4 v17, s[24:27], s3 offen offset:1024 lds\n"
    "buffer_load_dwordx4 v18, s[24:27], s3 offen offset:2048 lds\n"
    "buffer_load_dwordx4 v19, s[24:27], s3 offen offset:3072 lds\n"
    "s_add_u32 m0, s98, 32256\n"
    "s_and_b32 s3, s10, 0x7ffff\n"
    "s_add_u32 s10, s10, 65536\n"
    "buffer_load_dwordx4 v16, s[24:27], s3 offen offset:0 lds\n"
    "buffer_load_dwordx4 v17, s[24:27], s3 offen offset:1024 lds\n"
    "buffer_load_dwordx4 v18, s[24:27], s3 offen offset:2048 lds\n"
    "buffer_load_dwordx4 v19, s[24:27], s3 offen offset:3072 lds\n"
    "s_add_u32 m0, s98, 48640\n"
    "s_and_b32 s3, s10, 0x7ffff\n"
    "s_add_u32 s10, s10, 65536\n"
    "buffer_load_dwordx4 v16, s[24:27], s3 offen offset:0 lds\n"
    "buffer_load_dwordx4 v17, s[24:27], s3 offen offset:1024 lds\n"
    "buffer_load_dwordx4 v18, s[24:27], s3 offen offset:2048 lds\n"
    "buffer_load_dwordx4 v19, s[24:27], s3 offen offset:3072 lds\n"
    "s_add_u32 m0, s98, 81920\n"
    "s_and_b32 s3, s10, 0x7ffff\n"
    "s_add_u32 s10, s10, 65536\n"
    "buffer_load_dwordx4 v16, s[24:27], s3 offen offset:0 lds\n"
    "buffer_load_dwordx4 v17, s[24:27], s3 offen offset:1024 lds\n"
    "buffer_load_dwordx4 v18, s[24:27], s3 offen offset:2048 lds\n"
    "buffer_load_dwordx4 v19, s[24:27], s3 offen offset:3072 lds\n"
    "s_add_u32 m0, s98, 98304\n"
    "s_and_b32 s3, s10, 0x7ffff\n"
    "s_add_u32 s10, s10, 65536\n"
    "buffer_load_dwordx4 v16, s[24:27], s3 offen offset:0 lds\n"
    "buffer_load_dwordx4 v17, s[24:27], s3 offen offset:1024 lds\n"
    "buffer_load_dwordx4 v18, s[24:27], s3 offen offset:2048 lds\n"
    "buffer_load_dwordx4 v19, s[24:27], s3 offen offset:3072 lds\n"
    "s_movk_i32 s60, 12288\n"
    "buffer_load_dwordx4 v[68:71], v50, s[20:23], 0 offen sc1\n"
    "buffer_load_dwordx4 v[72:75], v50, s[20:23], s60 offen sc1\n"
    "s_lshl_b32 s60, s53, 8\n"
    "buffer_load_dwordx2 v[66:67], v108, s[28:31], s60 offen\n"
    "buffer_load_dwordx4 v[56:59], v11, s[12:15], 0 offen offset:0\n"
    "buffer_load_dwordx4 v[60:63], v11, s[12:15], 0 offen offset:32\n"
    "s_getpc_b64 s[60:61]\n"
    ".Lk_Lct0:\n"
    "s_sub_u32 s60, s60, .Lk_Lct0-.Lk_Lstart\n"
    "s_subb_u32 s61, s61, 0\n"
    "s_and_b32 s59, s57, 7\n"
    "v_lshl_add_u32 v14, v0, 3, s59\n"
    "v_lshlrev_b32 v14, 7, v14\n"
    "v_cmp_gt_u32 vcc, .Lk_Lend-.Lk_Lstart-4, v14\n"
    "s_nop 0\n"
    "s_and_b64 exec, vcc, -1\n"
    "s_cbranch_execz .Lk_Lct0x\n"
    "global_load_dword v104, v14, s[60:61]\n"
    ".Lk_Lct0x:\n"
    "s_mov_b64 exec, -1\n"
    "s_waitcnt vmcnt(29)\n"
    "v_mov_b32_e64 v14, 1\n"
    "s_mov_b64 exec, 1\n"
    "ds_add_u32 v14, v14 offset:13251\n"
    "s_mov_b64 exec, -1\n"
    "s_waitcnt lgkmcnt(0)\n"
    "s_mov_b32 s1, 8388608\n"
    "v_mov_b32 v14, 0\n"
    ".Lk_Lw8_1:\n"
    "s_sleep 1\n"
    "ds_read_b32 v44, v14 offset:13252\n"
    "s_sub_u32 s1, s1, 1\n"
    "s_cbranch_scc1 .Lk_Lx8_1\n"
    "s_waitcnt lgkmcnt(0)\n"
    "v_readfirstlane_b32 s60, v44\n"
    "s_waitcnt lgkmcnt(0)\n"
    "s_cmp_lt_u32 s60, 4\n"
    "s_cbranch_scc1 .Lk_Lw8_1\n"
    ".Lk_Lx8_1:\n"
    "s_waitcnt lgkmcnt(0)\n"
    "ds_read_b64_tr_b16 v[128:129], v101 offset:33280\n"
    "ds_read_b64_tr_b16 v[130:131], v101 offset:33792\n"
    "ds_read_b64_tr_b16 v[132:133], v101 offset:37376\n"
    "ds_read_b64_tr_b16 v[134:135], v101 offset:37888\n"
    "ds_read_b64_tr_b16 v[136:137], v101 offset:41472\n"
    "ds_read_b64_tr_b16 v[138:139], v101 offset:41984\n"
    "ds_read_b64_tr_b16 v[140:141], v101 offset:45568\n"
    "ds_read_b64_tr_b16 v[142:143], v101 offset:46080\n"
    "ds_read_b64_tr_b16 v[144:145], v101 offset:49664\n"
    "ds_read_b64_tr_b16 v[146:147], v101 offset:50176\n"
    "ds_read_b64_tr_b16 v[148:149], v101 offset:53760\n"
    "ds_read_b64_tr_b16 v[150:151], v101 offset:54272\n"
    "ds_read_b64_tr_b16 v[152:153], v101 offset:57856\n"
    "ds_read_b64_tr_b16 v[154:155], v101 offset:58368\n"
    "ds_read_b64_tr_b16 v[156:157], v101 offset:61952\n"
    "ds_read_b64_tr_b16 v[158:159], v101 offset:62464\n"
    "s_waitcnt vmcnt(5) lgkmcnt(0)\n"
    "s_barrier\n"
    "v_mov_b32_e64 v48, v103\n"
    "ds_read_b64_tr_b16 v[160:161], v102 offset:512\n"
    "ds_read_b64_tr_b16 v[162:163], v102 offset:1024\n"
    "ds_read_b64_tr_b16 v[164:165], v102 offset:4608\n"
    "ds_read_b64_tr_b16 v[166:167], v102 offset:5120\n"
    "s_waitcnt vmcnt(4)\n"
    "v_xor_b32 v68, v52, v68\n"
    "v_xor_b32 v69, v53, v69\n"
    "v_xor_b32 v70, v54, v70\n"
    "v_xor_b32_e64 v71, v55, v71\n"
    "ds_read_b64_tr_b16 v[168:169], v102 offset:8704\n"
    "ds_read_b64_tr_b16 v[170:171], v102 offset:9216\n"
    "ds_read_b64_tr_b16 v[172:173], v102 offset:12800\n"
    "ds_read_b64_tr_b16 v[174:175], v102 offset:13312\n"
    "s_waitcnt vmcnt(3)\n"
    "v_xor_b32 v72, v52, v72\n"
    "v_xor_b32_e64 v73, v53, v73\n"
    "ds_read_b64_tr_b16 v[176:177], v100 offset:0\n"
    "ds_read_b64_tr_b16 v[178:179], v100 offset:512\n"
    "ds_read_b64_tr_b16 v[180:181], v100 offset:4096\n"
    "ds_read_b64_tr_b16 v[182:183], v100 offset:4608\n"
    "v_xor_b32 v74, v54, v74\n"
    "v_xor_b32 v75, v55, v75\n"
    "ds_read_b64_tr_b16 v[184:185], v100 offset:8192\n"
    "ds_read_b64_tr_b16 v[186:187], v100 offset:8704\n"
    "ds_read_b64_tr_b16 v[188:189], v100 offset:12288\n"
    "ds_read_b64_tr_b16 v[190:191], v100 offset:12800\n"
    "v_or3_b32 v14, v68, v69, v70\n"
    "v_or3_b32 v14, v14, v71, v72\n"
    "ds_read_b64_tr_b16 v[192:193], v100 offset:16384\n"
    "ds_read_b64_tr_b16 v[194:195], v100 offset:16896\n"
    "ds_read_b64_tr_b16 v[196:197], v100 offset:20480\n"
    "ds_read_b64_tr_b16 v[198:199], v100 offset:20992\n"
    "v_or3_b32 v14, v14, v73, v74\n"
    "v_or_b32_e64 v14, v14, v75\n"
    "ds_read_b64_tr_b16 v[200:201], v100 offset:24576\n"
    "ds_read_b64_tr_b16 v[202:203], v100 offset:25088\n"
    "ds_read_b64_tr_b16 v[204:205], v100 offset:28672\n"
    "ds_read_b64_tr_b16 v[206:207], v100 offset:29184\n"
    "v_and_b32 v14, 0xff, v14\n"
    "v_cmp_ne_u32 vcc, 0, v14\n"
    "s_cbranch_vccnz .Lk_R_laten\n"
    ".Lk_B_laten:\n"
    "ds_read_b64_tr_b16 v[208:209], v100 offset:32768\n"
    "ds_read_b64_tr_b16 v[210:211], v100 offset:33280\n"
    "ds_read_b64_tr_b16 v[212:213], v100 offset:36864\n"
    "ds_read_b64_tr_b16 v[214:215], v100 offset:37376\n"
    "v_pk_add_f32 v[68:69], v[68:69], v[72:73]\n"
    "v_pk_add_f32 v[70:71], v[70:71], v[74:75]\n"
    "v_mov_b64 v[72:73], v[68:69]\n"
    "v_mov_b64_e64 v[74:75], v[70:71]\n"
    "s_cmp_eq_u32 s48, 2\n"
    "ds_read_b64_tr_b16 v[216:217], v100 offset:40960\n"
    "ds_read_b64_tr_b16 v[218:219], v100 offset:41472\n"
    "ds_read_b64_tr_b16 v[220:221], v100 offset:45056\n"
    "ds_read_b64_tr_b16 v[222:223], v100 offset:45568\n"
    "s_cselect_b32 s58, 1.0, 0\n"
    "s_cselect_b32 s59, 0, 1920\n"
    "v_permlane32_swap_b32 v68, v72\n"
    "v_permlane32_swap_b32 v69, v73\n"
    "v_permlane32_swap_b32 v70, v74\n"
    "ds_read_b64_tr_b16 v[224:225], v101 offset:512\n"
    "ds_read_b64_tr_b16 v[226:227], v101 offset:1024\n"
    "ds_read_b64_tr_b16 v[228:229], v101 offset:4608\n"
    "ds_read_b64_tr_b16 v[230:231], v101 offset:5120\n"
    "v_permlane32_swap_b32_e64 v71, v75\n"
    "v_pk_add_f32 v[68:69], v[68:69], v[72:73]\n"
    "v_pk_add_f32 v[70:71], v[70:71], v[74:75]\n"
    "ds_read_b64_tr_b16 v[232:233], v101 offset:8704\n"
    "ds_read_b64_tr_b16 v[234:235], v101 offset:9216\n"
    "ds_read_b64_tr_b16 v[236:237], v101 offset:12800\n"
    "ds_read_b64_tr_b16 v[238:239], v101 offset:13312\n"
    "v_cvt_pk_bf16_f32 v72, v68, v69\n"
    "v_cvt_pk_bf16_f32 v73, v70, v71\n"
    "v_lshlrev_b32_e64 v68, 16, v72\n"
    "ds_read_b64_tr_b16 v[240:241], v101 offset:16896\n"
    "ds_read_b64_tr_b16 v[242:243], v101 offset:17408\n"
    "ds_read_b64_tr_b16 v[244:245], v101 offset:20992\n"
    "ds_read_b64_tr_b16 v[246:247], v101 offset:21504\n"
    "v_and_b32 v69, 0xffff0000, v72\n"
    "v_lshlrev_b32_e64 v70, 16, v73\n"
    "v_and_b32 v71, 0xffff0000, v73\n"
    "ds_read_b64_tr_b16 v[248:249], v101 offset:25088\n"
    "ds_read_b64_tr_b16 v[250:251], v101 offset:25600\n"
    "ds_read_b64_tr_b16 v[252:253], v101 offset:29184\n"
    "ds_read_b64_tr_b16 v[254:255], v101 offset:29696\n"
    "v_add_f32 v68, s58, v68\n"
    "v_add_f32 v69, s58, v69\n"
    "v_add_f32 v70, s58, v70\n"
    "v_add_f32 v71, s58, v71\n"
    "v_cvt_pk_bf16_f32 v72, v68, v69\n"
    "v_cvt_pk_bf16_f32 v73, v70, v71\n"
    "v_lshlrev_b32_e64 v68, 16, v72\n"
    "v_and_b32 v69, 0xffff0000, v72\n"
    "v_lshlrev_b32_e64 v70, 16, v73\n"
    "v_and_b32 v71, 0xffff0000, v73\n"
    "s_waitcnt vmcnt(2)\n"
    "v_lshlrev_b32 v72, 16, v66\n"
    "v_and_b32 v73, 0xffff0000, v66\n"
    "v_lshlrev_b32_e64 v74, 16, v67\n"
    "v_and_b32 v75, 0xffff0000, v67\n"
    "v_pk_mul_f32 v[72:73], v[72:73], v[68:69]\n"
    "v_pk_mul_f32 v[74:75], v[74:75], v[70:71]\n"
    "v_pk_add_f32 v[76:77], v[72:73], v[74:75]\n"
    "v_add_f32 v76, v76, v77\n"
    "s_lshl_b32 s60, s48, 9\n"
    "v_lshl_add_u32 v14, v2, 4, s60\n"
    "v_lshl_add_u32 v78, v2, 4, s59\n"
    "v_add_f32_dpp v76, v76, v76 quad_perm:[1,0,3,2] row_mask:0xf bank_mask:0xf\n"
    "s_nop 1\n"
    "v_add_f32_dpp v76, v76, v76 quad_perm:[2,3,0,1] row_mask:0xf bank_mask:0xf\n"
    "s_nop 1\n"
    "v_add_f32_dpp v76, v76, v76 row_half_mirror row_mask:0xf bank_mask:0xf\n"
    "s_nop 1\n"
    "v_add_f32_dpp v76, v76, v76 row_mirror row_mask:0xf bank_mask:0xf\n"
    "v_mov_b32 v77, v76\n"
    "s_lshl_b32 s60, s48, 2\n"
    "v_mov_b32 v79, s60\n"
    "v_permlane16_swap_b32 v76, v77\n"
    "v_add_f32 v76, v76, v77\n"
    "ds_write_b128 v14, v[68:71] offset:8704\n"
    "ds_write_b128 v78, v[72:75] offset:10752\n"
    "ds_write_b32 v79, v76 offset:13184\n"
    "v_and_b32 v14, 1, v1\n"
    "v_lshlrev_b32 v14, 2, v14\n"
    "v_sub_u32_e64 v50, 8, v14\n"
    "s_waitcnt lgkmcnt(0)\n"
    "s_barrier\n"
    "ds_read_b128 v[80:83], v48 offset:8704\n"
    "ds_read_b128 v[84:87], v48 offset:8736\n"
    "ds_read_b128 v[88:91], v48 offset:8768\n"
    "ds_read_b128 v[92:95], v48 offset:8800\n"
    "ds_read_b128 v[96:99], v48 offset:10752\n"
    "ds_read_b128 v[100:103], v48 offset:10784\n"
    "s_waitcnt vmcnt(0)\n"
    "ds_read_b128 v[104:107], v48 offset:10816\n"
    "ds_read_b128 v[108:111], v48 offset:10848\n"
    "ds_read_b32 v51, v50 offset:13184\n"
    "v_permlane32_swap_b32 v56, v58\n"
    "v_permlane32_swap_b32 v57, v59\n"
    "v_permlane32_swap_b32 v60, v62\n"
    "v_permlane32_swap_b32 v61, v63\n"
    "v_lshlrev_b32 v32, 16, v56\n"
    "v_and_b32 v33, 0xffff0000, v56\n"
    "v_lshlrev_b32_e64 v34, 16, v57\n"
    "v_and_b32 v35, 0xffff0000, v57\n"
    "v_lshlrev_b32_e64 v36, 16, v58\n"
    "v_and_b32 v37, 0xffff0000, v58\n"
    "v_lshlrev_b32_e64 v38, 16, v59\n"
    "v_and_b32 v39, 0xffff0000, v59\n"
    "v_lshlrev_b32_e64 v40, 16, v60\n"
    "v_and_b32 v41, 0xffff0000, v60\n"
    "v_lshlrev_b32_e64 v42, 16, v61\n"
    "v_and_b32 v43, 0xffff0000, v61\n"
    "v_lshlrev_b32_e64 v44, 16, v62\n"
    "v_and_b32 v45, 0xffff0000, v62\n"
    "v_lshlrev_b32_e64 v46, 16, v63\n"
    "v_and_b32 v47, 0xffff0000, v63\n"
    "s_waitcnt lgkmcnt(0)\n"
    "s_barrier\n"
    "ds_read_b128 v[16:19], v13 offset:15872\n"
    "ds_read_b128 v[20:23], v13 offset:16896\n"
    "ds_read_b128 v[24:27], v13 offset:17920\n"
    "ds_read_b128 v[28:31], v13 offset:18944\n"
    "ds_read_b128 v[112:115], v13 offset:24064\n"
    "ds_read_b128 v[116:119], v13 offset:25088\n"
    "s_waitcnt lgkmcnt(5)\n"
    "v_mfma_f32_32x32x16_bf16 v[64:79], v[128:131], v[16:19], 0\n"
    "ds_read_b128 v[120:123], v13 offset:26112\n"
    "s_lshl_b32 s58, s48, 9\n"
    "v_lshl_add_u32 v14, v2, 4, s58\n"
    "s_waitcnt lgkmcnt(5)\n"
    "v_mfma_f32_32x32x16_bf16 v[64:79], v[132:135], v[20:23], v[64:79]\n"
    "ds_read_b128 v[124:127], v13 offset:27136\n"
    "v_lshlrev_b32 v61, 4, v2\n"
    "s_lshl2_add_u32 s63, s50, s53\n"
    "s_waitcnt lgkmcnt(5)\n"
    "v_mfma_f32_32x32x16_bf16 v[64:79], v[136:139], v[24:27], v[64:79]\n"
    "ds_read_b128 v[16:19], v13 offset:32256\n"
    "s_lshl_b32 s63, s63, 9\n"
    "s_add_u32 s63, s63, 327680\n"
    "s_waitcnt lgkmcnt(5)\n"
    "v_mfma_f32_32x32x16_bf16 v[64:79], v[140:143], v[28:31], v[64:79]\n"
    "ds_read_b128 v[20:23], v13 offset:33280\n"
    "v_lshl_add_u32 v15, v12, 10, v61\n"
    "s_lshl_b32 s62, s50, 11\n"
    "s_waitcnt lgkmcnt(5)\n"
    "v_mfma_f32_32x32x16_bf16 v[64:79], v[160:163], v[112:115], v[64:79]\n"
    "ds_read_b128 v[24:27], v13 offset:34304\n"
    "s_add_u32 s62, s62, 327680\n"
    "s_lshl_b32 s58, s52, 5\n"
    "s_waitcnt lgkmcnt(5)\n"
    "v_mfma_f32_32x32x16_bf16 v[64:79], v[164:167], v[116:119], v[64:79]\n"
    "ds_read_b128 v[28:31], v13 offset:35328\n"
    "s_sub_u32 s58, 2047, s58\n"
    "v_sub_u32 v60, s58, v2\n"
    "s_waitcnt lgkmcnt(5)\n"
    "v_mfma_f32_32x32x16_bf16 v[64:79], v[168:171], v[120:123], v[64:79]\n"
    "ds_read_b128 v[112:115], v13 offset:40448\n"
    "s_lshl_b32 s58, s50, 4\n"
    "s_add_u32 s58, s58, 524288\n"
    "s_waitcnt lgkmcnt(5)\n"
    "v_mfma_f32_32x32x16_bf16 v[64:79], v[172:175], v[124:127], v[64:79]\n"
    "ds_read_b128 v[116:119], v13 offset:41472\n"
    "s_lshl2_add_u32 s59, s53, s48\n"
    "s_cmp_eq_u32 s59, 4\n"
    "s_waitcnt lgkmcnt(5)\n"
    "v_mfma_f32_32x32x16_bf16 v[64:79], v[192:195], v[16:19], v[64:79]\n"
    "ds_read_b128 v[120:123], v13 offset:42496\n"
    "s_cselect_b32 s58, s58, 0x7f000000\n"
    "v_lshl_add_u32 v9, v1, 13, s58\n"
    "s_waitcnt lgkmcnt(5)\n"
    "v_mfma_f32_32x32x16_bf16 v[64:79], v[196:199], v[20:23], v[64:79]\n"
    "ds_read_b128 v[124:127], v13 offset:43520\n"
    "v_lshlrev_b32_e64 v8, 4, v1\n"
    "s_mov_b32 s12, 8388608\n"
    "s_waitcnt lgkmcnt(5)\n"
    "v_mfma_f32_32x32x16_bf16 v[64:79], v[200:203], v[24:27], v[64:79]\n"
    "ds_read_b128 v[16:19], v13 offset:19968\n"
    "s_mov_b32 s13, 10485760\n"
    "s_movk_i32 s14, 0x7ff\n"
    "s_waitcnt lgkmcnt(5)\n"
    "v_mfma_f32_32x32x16_bf16 v[64:79], v[204:207], v[28:31], v[64:79]\n"
    "ds_read_b128 v[20:23], v13 offset:20992\n"
    "s_movk_i32 s16, 272\n"
    "s_lshl_b32 s15, s56, 13\n"
    "s_waitcnt lgkmcnt(5)\n"
    "v_mfma_f32_32x32x16_bf16 v[64:79], v[224:227], v[112:115], v[64:79]\n"
    "ds_read_b128 v[24:27], v13 offset:22016\n"
    "s_add_u32 s15, s15, 753664\n"
    "s_waitcnt lgkmcnt(5)\n"
    "v_mfma_f32_32x32x16_bf16 v[64:79], v[228:231], v[116:119], v[64:79]\n"
    "ds_read_b128 v[28:31], v13 offset:23040\n"
    "s_waitcnt lgkmcnt(5)\n"
    "v_mfma_f32_32x32x16_bf16 v[64:79], v[232:235], v[120:123], v[64:79]\n"
    "ds_read_b128 v[112:115], v13 offset:28160\n"
    "s_waitcnt lgkmcnt(5)\n"
    "v_mfma_f32_32x32x16_bf16 v[64:79], v[236:239], v[124:127], v[64:79]\n"
    "ds_read_b128 v[116:119], v13 offset:29184\n"
    "s_waitcnt lgkmcnt(5)\n"
    "v_mfma_f32_32x32x16_bf16 v[64:79], v[144:147], v[16:19], v[64:79]\n"
    "ds_read_b128 v[120:123], v13 offset:30208\n"
    "s_waitcnt lgkmcnt(5)\n"
    "v_mfma_f32_32x32x16_bf16 v[64:79], v[148:151], v[20:23], v[64:79]\n"
    "ds_read_b128 v[124:127], v13 offset:31232\n"
    "s_waitcnt lgkmcnt(5)\n"
    "v_mfma_f32_32x32x16_bf16 v[64:79], v[152:155], v[24:27], v[64:79]\n"
    "ds_read_b128 v[16:19], v13 offset:36352\n"
    "s_waitcnt lgkmcnt(5)\n"
    "v_mfma_f32_32x32x16_bf16 v[64:79], v[156:159], v[28:31], v[64:79]\n"
    "ds_read_b128 v[20:23], v13 offset:37376\n"
    "s_waitcnt lgkmcnt(5)\n"
    "v_mfma_f32_32x32x16_bf16 v[64:79], v[176:179], v[112:115], v[64:79]\n"
    "ds_read_b128 v[24:27], v13 offset:38400\n"
    "s_waitcnt lgkmcnt(5)\n"
    "v_mfma_f32_32x32x16_bf16 v[64:79], v[180:183], v[116:119], v[64:79]\n"
    "ds_read_b128 v[28:31], v13 offset:39424\n"
    "s_waitcnt lgkmcnt(5)\n"
    "v_mfma_f32_32x32x16_bf16 v[64:79], v[184:187], v[120:123], v[64:79]\n"
    "ds_read_b128 v[112:115], v13 offset:44544\n"
    "s_waitcnt lgkmcnt(5)\n"
    "v_mfma_f32_32x32x16_bf16 v[64:79], v[188:191], v[124:127], v[64:79]\n"
    "ds_read_b128 v[116:119], v13 offset:45568\n"
    "s_waitcnt lgkmcnt(5)\n"
    "v_mfma_f32_32x32x16_bf16 v[64:79], v[208:211], v[16:19], v[64:79]\n"
    "ds_read_b128 v[120:123], v13 offset:46592\n"
    "s_waitcnt lgkmcnt(5)\n"
    "v_mfma_f32_32x32x16_bf16 v[64:79], v[212:215], v[20:23], v[64:79]\n"
    "ds_read_b128 v[124:127], v13 offset:47616\n"
    "s_waitcnt lgkmcnt(5)\n"
    "v_mfma_f32_32x32x16_bf16 v[64:79], v[216:219], v[24:27], v[64:79]\n"
    "s_waitcnt lgkmcnt(4)\n"
    "v_mfma_f32_32x32x16_bf16 v[64:79], v[220:223], v[28:31], v[64:79]\n"
    "s_waitcnt lgkmcnt(3)\n"
    "v_mfma_f32_32x32x16_bf16 v[64:79], v[240:243], v[112:115], v[64:79]\n"
    "s_waitcnt lgkmcnt(2)\n"
    "v_mfma_f32_32x32x16_bf16 v[64:79], v[244:247], v[116:119], v[64:79]\n"
    "s_waitcnt lgkmcnt(1)\n"
    "v_mfma_f32_32x32x16_bf16 v[64:79], v[248:251], v[120:123], v[64:79]\n"
    "s_waitcnt lgkmcnt(0)\n"
    "v_mfma_f32_32x32x16_bf16 v[64:79], v[252:255], v[124:127], v[64:79]\n"
    "s_nop 11\n"
    "v_pk_fma_f32 v[32:33], v[80:81], v[64:65], v[32:33]\n"
    "v_pk_fma_f32 v[34:35], v[82:83], v[66:67], v[34:35]\n"
    "v_pk_fma_f32 v[36:37], v[84:85], v[68:69], v[36:37]\n"
    "v_pk_fma_f32 v[38:39], v[86:87], v[70:71], v[38:39]\n"
    "v_pk_fma_f32 v[40:41], v[88:89], v[72:73], v[40:41]\n"
    "v_pk_fma_f32 v[42:43], v[90:91], v[74:75], v[42:43]\n"
    "v_pk_fma_f32 v[44:45], v[92:93], v[76:77], v[44:45]\n"
    "v_pk_fma_f32 v[46:47], v[94:95], v[78:79], v[46:47]\n"
    "v_pk_add_f32 v[56:57], v[32:33], v[34:35]\n"
    "v_pk_mul_f32 v[58:59], v[32:33], v[32:33]\n"
    "v_pk_mul_f32 v[62:63], v[32:33], v[96:97]\n"
    "v_pk_fma_f32 v[58:59], v[34:35], v[34:35], v[58:59]\n"
    "v_pk_fma_f32 v[62:63], v[34:35], v[98:99], v[62:63]\n"
    "v_pk_add_f32 v[56:57], v[56:57], v[36:37]\n"
    "v_pk_fma_f32 v[58:59], v[36:37], v[36:37], v[58:59]\n"
    "v_pk_fma_f32 v[62:63], v[36:37], v[100:101], v[62:63]\n"
    "v_pk_add_f32 v[56:57], v[56:57], v[38:39]\n"
    "v_pk_fma_f32 v[58:59], v[38:39], v[38:39], v[58:59]\n"
    "v_pk_fma_f32 v[62:63], v[38:39], v[102:103], v[62:63]\n"
    "v_pk_add_f32 v[56:57], v[56:57], v[40:41]\n"
    "v_pk_fma_f32 v[58:59], v[40:41], v[40:41], v[58:59]\n"
    "v_pk_fma_f32 v[62:63], v[40:41], v[104:105], v[62:63]\n"
    "v_pk_add_f32 v[56:57], v[56:57], v[42:43]\n"
    "v_pk_fma_f32 v[58:59], v[42:43], v[42:43], v[58:59]\n"
    "v_pk_fma_f32 v[62:63], v[42:43], v[106:107], v[62:63]\n"
    "v_pk_add_f32 v[56:57], v[56:57], v[44:45]\n"
    "v_pk_fma_f32 v[58:59], v[44:45], v[44:45], v[58:59]\n"
    "v_pk_fma_f32 v[62:63], v[44:45], v[108:109], v[62:63]\n"
    "v_pk_add_f32 v[56:57], v[56:57], v[46:47]\n"
    "v_pk_fma_f32 v[58:59], v[46:47], v[46:47], v[58:59]\n"
    "v_pk_fma_f32 v[62:63], v[46:47], v[110:111], v[62:63]\n"
    "v_add_f32 v128, v56, v57\n"
    "v_add_f32 v129, v58, v59\n"
    "v_add_f32 v130, v62, v63\n"
    "v_add_f32 v132, v56, v57\n"
    "v_add_f32 v133, v58, v59\n"
    "v_add_f32 v134, v62, v63\n"
    "v_permlane32_swap_b32 v128, v132\n"
    "v_permlane32_swap_b32 v129, v133\n"
    "v_permlane32_swap_b32_e64 v130, v134\n"
    "v_pk_add_f32 v[128:129], v[128:129], v[132:133]\n"
    "v_pk_add_f32 v[130:131], v[130:131], v[134:135]\n"
    "s_cmp_eq_u32 s48, 0\n"
    "ds_write_b128 v14, v[128:131] offset:5120\n"
    "s_waitcnt lgkmcnt(0)\n"
    "s_barrier\n"
    "s_cbranch_scc0 .Lk_Lnostn\n"
    "ds_read_b128 v[128:131], v61 offset:5120\n"
    "ds_read_b128 v[132:135], v61 offset:5632\n"
    "ds_read_b128 v[136:139], v61 offset:6144\n"
    "ds_read_b128 v[140:143], v61 offset:6656\n"
    "s_waitcnt lgkmcnt(2)\n"
    "v_pk_add_f32 v[128:129], v[128:129], v[132:133]\n"
    "v_pk_add_f32 v[130:131], v[130:131], v[134:135]\n"
    "s_waitcnt lgkmcnt(0)\n"
    "v_pk_add_f32 v[136:137], v[136:137], v[140:141]\n"
    "v_pk_add_f32 v[138:139], v[138:139], v[142:143]\n"
    "v_pk_add_f32 v[128:129], v[128:129], v[136:137]\n"
    "v_pk_add_f32 v[130:131], v[130:131], v[138:139]\n"
    "v_and_or_b32 v128, v128, s11, v52\n"
    "v_and_or_b32 v129, v129, s11, v53\n"
    "v_and_or_b32 v130, v130, s11, v54\n"
    "v_and_or_b32 v131, v51, s11, v55\n"
    "s_mov_b32 exec_hi, 0\n"
    "buffer_store_dwordx4 v[128:131], v61, s[20:23], s63 offen sc1\n"
    "s_mov_b64 exec, -1\n"
    ".Lk_Lnostn:\n"
    "ds_read_b128 v[208:211], v48 offset:9216\n"
    "ds_read_b128 v[212:215], v48 offset:9248\n"
    "ds_read_b128 v[216:219], v48 offset:9280\n"
    "ds_read_b128 v[220:223], v48 offset:9312\n"
    "ds_read_b128 v[192:195], v48 offset:9728\n"
    "ds_read_b128 v[196:199], v48 offset:9760\n"
    "ds_read_b128 v[200:203], v48 offset:9792\n"
    "ds_read_b128 v[204:207], v48 offset:9824\n"
    "ds_read_b128 v[16:19], v48 offset:10240\n"
    "ds_read_b128 v[20:23], v48 offset:10272\n"
    "ds_read_b128 v[24:27], v48 offset:10304\n"
    "ds_read_b128 v[28:31], v48 offset:10336\n"
    "s_sleep 12\n"
    "buffer_load_dwordx4 v[128:131], v15, s[20:23], s62 offen offset:0 sc1\n"
    "buffer_load_dwordx4 v[132:135], v15, s[20:23], s62 offen offset:512 sc1\n"
    "s_waitcnt vmcnt(1)\n"
    "v_xor_b32 v128, v52, v128\n"
    "v_xor_b32 v129, v53, v129\n"
    "v_xor_b32 v130, v54, v130\n"
    "v_xor_b32 v131, v55, v131\n"
    "v_or_b32_e64 v14, v128, v129\n"
    "v_or3_b32 v14, v14, v130, v131\n"
    "s_waitcnt vmcnt(0)\n"
    "v_xor_b32 v132, v52, v132\n"
    "v_xor_b32 v133, v53, v133\n"
    "v_xor_b32 v134, v54, v134\n"
    "v_xor_b32_e64 v135, v55, v135\n"
    "v_or3_b32 v14, v14, v132, v133\n"
    "v_or3_b32 v14, v14, v134, v135\n"
    "v_and_b32 v14, 0xff, v14\n"
    "v_cmp_ne_u32 vcc, 0, v14\n"
    "s_cbranch_vccnz .Lk_R_stn\n"
    ".Lk_B_stn:\n"
    "v_pk_add_f32 v[136:137], v[128:129], v[132:133]\n"
    "v_pk_add_f32 v[138:139], v[130:131], v[134:135]\n"
    "v_pk_add_f32 v[128:129], v[128:129], v[132:133]\n"
    "v_pk_add_f32 v[130:131], v[130:131], v[134:135]\n"
    "s_nop 0\n"
    "v_permlane32_swap_b32 v128, v136\n"
    "v_permlane32_swap_b32 v129, v137\n"
    "v_permlane32_swap_b32 v130, v138\n"
    "v_permlane32_swap_b32_e64 v131, v139\n"
    "v_pk_add_f32 v[128:129], v[128:129], v[136:137]\n"
    "v_pk_add_f32 v[130:131], v[130:131], v[138:139]\n"
    "v_mul_f32 v132, 0x3b000000, v128\n"
    "v_mul_f32 v129, 0x3b000000, v129\n"
    "v_fma_f32 v129, -v132, v132, v129\n"
    "v_max_f32_e64 v129, 0, v129\n"
    "v_cvt_pk_bf16_f32 v129, 0, v129\n"
    "v_cvt_pk_bf16_f32 v132, 0, v132\n"
    "v_add_f32 v129, 0x3727c5ac, v129\n"
    "v_cvt_pk_bf16_f32 v129, 0, v129\n"
    "v_sqrt_f32 v129, v129\n"
    "v_mov_b32_dpp v136, v131 quad_perm:[0,0,2,2] row_mask:0xf bank_mask:0xf\n"
    "v_cvt_pk_bf16_f32 v129, 0, v129\n"
    "v_rcp_f32 v133, v129\n"
    "v_mov_b32_dpp v137, v131 quad_perm:[1,1,3,3] row_mask:0xf bank_mask:0xf\n"
    "v_fma_f32 v130, -v132, v136, v130\n"
    "v_fma_f32 v134, v130, v133, v137\n"
    "v_mul_f32 v132, -v132, v133\n"
    "v_ashrrev_i32 v138, 31, v134\n"
    "v_or_b32 v138, 0x80000000, v138\n"
    "v_xor_b32 v138, v138, v134\n"
    "v_bfi_b32 v138, s14, v60, v138\n"
    "s_nop 1\n"
    "v_min_u32_dpp v143, v138, v138 quad_perm:[1,0,3,2] row_mask:0xf bank_mask:0xf\n"
    "v_max_u32_dpp v142, v138, v138 quad_perm:[1,0,3,2] row_mask:0xf bank_mask:0xf\n"
    "s_nop 1\n"
    "v_min_u32_dpp v146, v142, v142 quad_perm:[2,3,0,1] row_mask:0xf bank_mask:0xf\n"
    "v_max_u32_dpp v142, v142, v142 quad_perm:[2,3,0,1] row_mask:0xf bank_mask:0xf\n"
    "v_max_u32_dpp v147, v143, v143 quad_perm:[2,3,0,1] row_mask:0xf bank_mask:0xf\n"
    "v_max_u32_e64 v143, v146, v147\n"
    "v_min_u32_dpp v146, v142, v142 row_half_mirror row_mask:0xf bank_mask:0xf\n"
    "v_max_u32_dpp v142, v142, v142 row_half_mirror row_mask:0xf bank_mask:0xf\n"
    "v_max_u32_dpp v147, v143, v143 row_half_mirror row_mask:0xf bank_mask:0xf\n"
    "v_max_u32_e64 v143, v146, v147\n"
    "v_min_u32_dpp v146, v142, v142 row_mirror row_mask:0xf bank_mask:0xf\n"
    "v_max_u32_dpp v142, v142, v142 row_mirror row_mask:0xf bank_mask:0xf\n"
    "v_max_u32_dpp v147, v143, v143 row_mirror row_mask:0xf bank_mask:0xf\n"
    "v_max_u32 v143, v146, v147\n"
    "v_mov_b32 v146, v142\n"
    "v_mov_b32 v147, v143\n"
    "s_nop 0\n"
    "v_permlane16_swap_b32 v142, v146\n"
    "v_permlane16_swap_b32 v143, v147\n"
    "v_min_u32 v148, v142, v146\n"
    "v_max_u32 v142, v142, v146\n"
    "v_max3_u32 v143, v143, v147, v148\n"
    "v_add3_u32 v144, v142, v143, s8\n"
    "v_cmp_ge_u32 s[68:69], v138, v143\n"
    "s_mov_b32 exec_hi, 15\n"
    "buffer_store_dwordx3 v[142:144], v9, s[20:23], 0 offen sc1\n"
    "s_mov_b64 exec, -1\n"
    "v_pk_fma_f32 v[64:65], v[32:33], v[132:133], v[132:133] op_sel:[0,1,0] op_sel_hi:[1,1,0]\n"
    "v_pk_fma_f32 v[66:67], v[34:35], v[132:133], v[132:133] op_sel:[0,1,0] op_sel_hi:[1,1,0]\n"
    "v_pk_fma_f32 v[68:69], v[36:37], v[132:133], v[132:133] op_sel:[0,1,0] op_sel_hi:[1,1,0]\n"
    "v_pk_fma_f32 v[70:71], v[38:39], v[132:133], v[132:133] op_sel:[0,1,0] op_sel_hi:[1,1,0]\n"
    "v_pk_fma_f32 v[72:73], v[40:41], v[132:133], v[132:133] op_sel:[0,1,0] op_sel_hi:[1,1,0]\n"
    "v_pk_fma_f32 v[74:75], v[42:43], v[132:133], v[132:133] op_sel:[0,1,0] op_sel_hi:[1,1,0]\n"
    "v_pk_fma_f32 v[76:77], v[44:45], v[132:133], v[132:133] op_sel:[0,1,0] op_sel_hi:[1,1,0]\n"
    "v_pk_fma_f32 v[78:79], v[46:47], v[132:133], v[132:133] op_sel:[0,1,0] op_sel_hi:[1,1,0]\n"
    "s_waitcnt lgkmcnt(7)\n"
    "v_pk_fma_f32 v[64:65], v[64:65], v[192:193], v[208:209]\n"
    "v_pk_fma_f32 v[66:67], v[66:67], v[194:195], v[210:211]\n"
    "s_waitcnt lgkmcnt(6)\n"
    "v_pk_fma_f32 v[68:69], v[68:69], v[196:197], v[212:213]\n"
    "v_pk_fma_f32 v[70:71], v[70:71], v[198:199], v[214:215]\n"
    "s_waitcnt lgkmcnt(5)\n"
    "v_pk_fma_f32 v[72:73], v[72:73], v[200:201], v[216:217]\n"
    "v_pk_fma_f32 v[74:75], v[74:75], v[202:203], v[218:219]\n"
    "s_waitcnt lgkmcnt(4)\n"
    "v_pk_fma_f32 v[76:77], v[76:77], v[204:205], v[220:221]\n"
    "v_pk_fma_f32 v[78:79], v[78:79], v[206:207], v[222:223]\n"
    "v_cvt_pk_bf16_f32 v120, v64, v65\n"
    "v_cvt_pk_bf16_f32 v121, v66, v67\n"
    "v_cvt_pk_bf16_f32 v122, v68, v69\n"
    "v_cvt_pk_bf16_f32 v123, v70, v71\n"
    "v_cvt_pk_bf16_f32 v124, v72, v73\n"
    "v_cvt_pk_bf16_f32 v125, v74, v75\n"
    "v_cvt_pk_bf16_f32 v126, v76, v77\n"
    "v_cvt_pk_bf16_f32 v127, v78, v79\n"
    "v_permlane32_swap_b32 v120, v122\n"
    "v_permlane32_swap_b32 v121, v123\n"
    "v_permlane32_swap_b32 v124, v126\n"
    "v_permlane32_swap_b32 v125, v127\n"
    "v_and_b32 v14, 0xff, v11\n"
    "v_cmp_eq_u32 vcc, v138, v142\n"
    "v_add_u32 v15, 256, v14\n"
    "v_mov_b32 v3, 1\n"
    "v_cndmask_b32 v14, v15, v14, vcc\n"
    "s_mov_b64 exec, s[68:69]\n"
    "ds_write_b128 v14, v[120:123] offset:12672\n"
    "ds_write_b128 v14, v[124:127] offset:12704\n"
    "s_mov_b64 exec, 1\n"
    "ds_add_u32 v3, v3 offset:13291\n"
    "s_mov_b64 exec, -1\n"
    "s_cmp_eq_u32 s48, 3\n"
    "s_cbranch_scc0 .Lk_Lnocand\n"
    "v_cmp_gt_u32 vcc, 16, v1\n"
    "v_and_b32 v4, 0xff, v13\n"
    "s_lshl_b32 s58, s53, 8\n"
    "v_cndmask_b32 v15, v143, v142, vcc\n"
    "v_bfi_b32 v15, v15, 0, s14\n"
    "v_lshl_add_u32 v15, v15, 10, s58\n"
    "v_add_u32 v15, v15, v4\n"
    "v_lshrrev_b32 v10, 2, v15\n"
    "s_waitcnt lgkmcnt(0)\n"
    "s_mov_b32 s1, 8388608\n"
    "v_mov_b32_e64 v14, 0\n"
    ".Lk_Lw8_11:\n"
    "ds_read_b32 v4, v14 offset:13292\n"
    "ds_read_b128 v[120:123], v61 offset:12672\n"
    "s_sub_u32 s1, s1, 1\n"
    "s_cbranch_scc1 .Lk_Lx8_11\n"
    "s_waitcnt lgkmcnt(1)\n"
    "v_readfirstlane_b32 s60, v4\n"
    "s_waitcnt lgkmcnt(0)\n"
    "s_cmp_lt_u32 s60, 4\n"
    "s_cbranch_scc1 .Lk_Lw8_11\n"
    ".Lk_Lx8_11:\n"
    "s_waitcnt lgkmcnt(0)\n"
    "v_add3_u32 v124, v120, v121, s8\n"
    "s_mov_b32 exec_hi, 0\n"
    "buffer_store_dwordx4 v[120:123], v15, s[20:23], s12 offen sc1\n"
    "v_add3_u32 v124, v124, v122, v123\n"
    "buffer_store_dword v124, v10, s[20:23], s13 offen sc1\n"
    "s_mov_b64 exec, -1\n"
    ".Lk_Lnocand:\n"
    "s_waitcnt lgkmcnt(6)\n"
    "v_pk_fma_f32 v[32:33], v[16:17], v[64:65], v[32:33]\n"
    "v_pk_fma_f32 v[34:35], v[18:19], v[66:67], v[34:35]\n"
    "s_waitcnt lgkmcnt(5)\n"
    "v_pk_fma_f32 v[36:37], v[20:21], v[68:69], v[36:37]\n"
    "v_pk_fma_f32 v[38:39], v[22:23], v[70:71], v[38:39]\n"
    "s_waitcnt lgkmcnt(4)\n"
    "v_pk_fma_f32 v[40:41], v[24:25], v[72:73], v[40:41]\n"
    "v_pk_fma_f32 v[42:43], v[26:27], v[74:75], v[42:43]\n"
    "s_waitcnt lgkmcnt(3)\n"
    "v_pk_fma_f32 v[44:45], v[28:29], v[76:77], v[44:45]\n"
    "v_pk_fma_f32 v[46:47], v[30:31], v[78:79], v[46:47]\n"
    "v_cvt_pk_bf16_f32 v80, v32, v33\n"
    "v_cvt_pk_bf16_f32 v81, v34, v35\n"
    "v_cvt_pk_bf16_f32 v82, v36, v37\n"
    "v_cvt_pk_bf16_f32 v83, v38, v39\n"
    "v_cvt_pk_bf16_f32 v84, v40, v41\n"
    "v_cvt_pk_bf16_f32 v85, v42, v43\n"
    "v_cvt_pk_bf16_f32 v86, v44, v45\n"
    "v_cvt_pk_bf16_f32 v87, v46, v47\n"
    "v_permlane32_swap_b32 v80, v82\n"
    "v_permlane32_swap_b32 v81, v83\n"
    "v_permlane32_swap_b32 v84, v86\n"
    "v_permlane32_swap_b32 v85, v87\n"
    "v_and_b32 v14, 0xff, v11\n"
    "v_mad_u32_u24 v14, v2, s16, v14\n"
    "ds_write_b128 v14, v[80:83] offset:0\n"
    "ds_write_b128 v14, v[84:87] offset:32\n"
    "v_lshrrev_b32_e64 v15, 4, v0\n"
    "v_and_b32 v14, 0xff, v13\n"
    "v_lshl_add_u32 v3, v15, 10, v14\n"
    "v_mad_u32_u24 v14, v15, s16, v14\n"
    "s_lshl_b32 s58, s52, 7\n"
    "s_add_u32 s58, s58, s53\n"
    "s_lshl_b32 s58, s58, 8\n"
    "s_add_u32 s59, s58, 16384\n"
    "s_cmp_lt_i32 s54, 0\n"
    "s_waitcnt lgkmcnt(0)\n"
    "s_barrier\n"
    "s_cbranch_scc0 .Lk_Rffw\n"
    "ds_read_b128 v[80:83], v14 offset:0\n"
    "ds_read_b128 v[84:87], v14 offset:4352\n"
    "s_waitcnt lgkmcnt(1)\n"
    "buffer_store_dwordx4 v[80:83], v3, s[32:35], s58 offen sc1\n"
    "s_waitcnt lgkmcnt(0)\n"
    "buffer_store_dwordx4 v[84:87], v3, s[32:35], s59 offen sc1\n"
    "s_mov_b32 s62, s15\n"
    "s_sleep 28\n"
    "buffer_load_dwordx3 v[144:146], v8, s[20:23], s62 offen sc1\n"
    "s_waitcnt vmcnt(0)\n"
    "v_add3_u32 v14, v144, v145, s8\n"
    "v_cmp_ne_u32 vcc, v14, v146\n"
    "s_cbranch_vccnz .Lk_R_kb\n"
    ".Lk_B_kb:\n"
    "v_min_u32_dpp v150, v144, v144 quad_perm:[1,0,3,2] row_mask:0xf bank_mask:0xf\n"
    "v_max_u32_dpp v148, v144, v144 quad_perm:[1,0,3,2] row_mask:0xf bank_mask:0xf\n"
    "v_max_u32_dpp v151, v145, v145 quad_perm:[1,0,3,2] row_mask:0xf bank_mask:0xf\n"
    "v_max_u32_e64 v149, v150, v151\n"
    "v_min_u32_dpp v150, v148, v148 quad_perm:[2,3,0,1] row_mask:0xf bank_mask:0xf\n"
    "v_max_u32_dpp v148, v148, v148 quad_perm:[2,3,0,1] row_mask:0xf bank_mask:0xf\n"
    "v_max_u32_dpp v151, v149, v149 quad_perm:[2,3,0,1] row_mask:0xf bank_mask:0xf\n"
    "v_max_u32_e64 v149, v150, v151\n"
    "v_min_u32_dpp v150, v148, v148 row_half_mirror row_mask:0xf bank_mask:0xf\n"
    "v_max_u32_dpp v148, v148, v148 row_half_mirror row_mask:0xf bank_mask:0xf\n"
    "v_max_u32_dpp v151, v149, v149 row_half_mirror row_mask:0xf bank_mask:0xf\n"
    "v_max_u32_e64 v149, v150, v151\n"
    "v_min_u32_dpp v150, v148, v148 row_mirror row_mask:0xf bank_mask:0xf\n"
    "v_max_u32_dpp v148, v148, v148 row_mirror row_mask:0xf bank_mask:0xf\n"
    "v_max_u32_dpp v151, v149, v149 row_mirror row_mask:0xf bank_mask:0xf\n"
    "v_max_u32 v149, v150, v151\n"
    "v_mov_b32 v150, v148\n"
    "v_mov_b32 v151, v149\n"
    "s_nop 0\n"
    "v_permlane16_swap_b32 v148, v150\n"
    "v_permlane16_swap_b32 v149, v151\n"
    "v_min_u32 v152, v148, v150\n"
    "v_max_u32 v148, v148, v150\n"
    "v_max3_u32 v149, v149, v151, v152\n"
    "v_mov_b32 v150, v148\n"
    "v_mov_b32 v151, v149\n"
    "s_nop 0\n"
    "v_permlane32_swap_b32 v148, v150\n"
    "v_permlane32_swap_b32 v149, v151\n"
    "v_min_u32 v152, v148, v150\n"
    "v_max_u32_e64 v148, v148, v150\n"
    "v_max3_u32 v149, v149, v151, v152\n"
    "s_barrier\n"
    ".Lk_Bffw:\n"
    "v_cmp_eq_u32 s[64:65], v148, v138\n"
    "v_cmp_eq_u32 s[66:67], v149, v138\n"
    "v_mul_f32 v88, 0xbfb8aa3b, v134\n"
    "v_exp_f32 v88, v88\n"
    "s_or_b64 s[68:69], s[64:65], s[66:67]\n"
    "s_cbranch_scc0 .Lk_Lexitn\n"
    "v_add_u32 v14, 512, v48\n"
    "v_add_f32 v88, 1.0, v88\n"
    "v_rcp_f32 v88, v88\n"
    "v_cndmask_b32 v14, v48, v14, s[66:67]\n"
    "s_mov_b64 exec, s[68:69]\n"
    "ds_write_b128 v14, v[32:35] offset:11264\n"
    "ds_write_b128 v14, v[36:39] offset:11296\n"
    "ds_write_b128 v14, v[40:43] offset:11328\n"
    "ds_write_b128 v14, v[44:47] offset:11360\n"
    "s_mov_b64 exec, -1\n"
    "v_lshrrev_b32_e64 v14, 3, v1\n"
    "v_and_b32 v15, 0x7f, v13\n"
    "v_lshl_add_u32 v8, v14, 12, v15\n"
    "s_lshl2_add_u32 s58, s53, s48\n"
    "s_lshl_b32 s58, s58, 8\n"
    "s_add_u32 s58, s58, 163840\n"
    "v_add_u32 v8, s58, v8\n"
    "v_add_u32 v89, -1, v12\n"
    "v_lshl_add_u32 v9, v12, 30, v8\n"
    "v_add_u32 v9, 98304, v9\n"
    "s_lshl_b32 s58, s48, 7\n"
    "v_lshl_add_u32 v63, v12, 3, v15\n"
    "v_add_u32 v63, s58, v63\n"
    "v_lshrrev_b32_e64 v62, 1, v63\n"
    "s_mov_b32 s62, 32768\n"
    "s_mov_b32 s63, 65536\n"
    "v_min_u32 v15, 27, v1\n"
    "v_lshlrev_b32 v15, 4, v15\n"
    "s_lshl2_add_u32 s58, s53, s48\n"
    "s_lshl_b32 s58, s58, 12\n"
    "s_add_u32 s58, s58, 851968\n"
    "s_mov_b64 exec, 0x0fffffff\n"
    "v_mov_b32_e64 v14, 0\n"
    "buffer_atomic_add v14, v15, s[20:23], s58 offen sc0 sc1\n"
    "s_waitcnt vmcnt(0)\n"
    "v_cmp_ne_u32 vcc, s8, v14\n"
    "s_mov_b64 exec, -1\n"
    "s_cbranch_vccnz .Lk_R_h\n"
    ".Lk_B_h:\n"
    "ds_read_b64 v[98:99], v63 offset:10240\n"
    "s_cmp_eq_u64 s[64:65], 0\n"
    "s_cbranch_scc1 .Lk_Lw0\n"
    "buffer_load_dwordx4 v[128:131], v8, s[20:23], 0 offen offset:0 sc1\n"
    "buffer_load_dwordx4 v[132:135], v8, s[20:23], s62 offen offset:0 sc1\n"
    "buffer_load_dwordx4 v[136:139], v8, s[20:23], s63 offen offset:0 sc1\n"
    "buffer_load_dwordx4 v[140:143], v9, s[20:23], 0 offen offset:0 sc1\n"
    "s_ff1_i32_b64 s59, s[64:65]\n"
    "v_readlane_b32 s58, v88, s59\n"
    "ds_read_b64 v[92:93], v63 offset:11264\n"
    "s_lshl_b32 s3, s52, 5\n"
    "s_add_u32 s3, s3, s59\n"
    "s_lshl2_add_u32 s3, s3, s53\n"
    "s_lshl_b32 s3, s3, 8\n"
    "s_waitcnt lgkmcnt(1)\n"
    "v_mul_f32 v96, s58, v98\n"
    "v_mul_f32 v97, s58, v99\n"
    "s_waitcnt vmcnt(3)\n"
    "v_xor_b32 v128, v52, v128\n"
    "v_xor_b32 v129, v53, v129\n"
    "v_xor_b32 v130, v54, v130\n"
    "v_xor_b32 v131, v55, v131\n"
    "v_or_b32 v14, v128, v129\n"
    "v_or3_b32 v14, v14, v130, v131\n"
    "s_waitcnt vmcnt(2)\n"
    "v_xor_b32 v132, v52, v132\n"
    "v_xor_b32 v133, v53, v133\n"
    "v_xor_b32 v134, v54, v134\n"
    "v_xor_b32_e64 v135, v55, v135\n"
    "v_or3_b32 v14, v14, v132, v133\n"
    "v_or3_b32 v14, v14, v134, v135\n"
    "s_waitcnt vmcnt(1)\n"
    "v_xor_b32 v136, v52, v136\n"
    "v_xor_b32 v137, v53, v137\n"
    "v_xor_b32 v138, v54, v138\n"
    "v_xor_b32_e64 v139, v55, v139\n"
    "v_or3_b32 v14, v14, v136, v137\n"
    "v_or3_b32 v14, v14, v138, v139\n"
    "s_waitcnt vmcnt(0)\n"
    "v_xor_b32 v140, v52, v140\n"
    "v_xor_b32 v141, v53, v141\n"
    "v_xor_b32 v142, v54, v142\n"
    "v_xor_b32 v143, v55, v143\n"
    "v_and_b32 v140, v89, v140\n"
    "v_and_b32 v141, v89, v141\n"
    "v_and_b32 v142, v89, v142\n"
    "v_and_b32_e64 v143, v89, v143\n"
    "v_or3_b32 v14, v14, v140, v141\n"
    "v_or3_b32 v14, v14, v142, v143\n"
    "v_and_b32 v14, 0xff, v14\n"
    "v_cmp_ne_u32 vcc, 0, v14\n"
    "s_cbranch_vccnz .Lk_R_f0\n"
    ".Lk_B_f0:\n"
    "v_pk_add_f32 v[128:129], v[128:129], v[132:133]\n"
    "v_pk_add_f32 v[130:131], v[130:131], v[134:135]\n"
    "v_pk_add_f32 v[136:137], v[136:137], v[140:141]\n"
    "v_pk_add_f32 v[138:139], v[138:139], v[142:143]\n"
    "v_pk_add_f32 v[128:129], v[128:129], v[136:137]\n"
    "v_pk_add_f32 v[130:131], v[130:131], v[138:139]\n"
    "s_nop 1\n"
    "v_permlane32_swap_b32 v128, v130\n"
    "v_permlane32_swap_b32_e64 v129, v131\n"
    "v_pk_add_f32 v[132:133], v[128:129], v[130:131]\n"
    "v_pk_add_f32 v[128:129], v[128:129], v[130:131]\n"
    "s_nop 1\n"
    "v_permlane16_swap_b32 v128, v132\n"
    "v_permlane16_swap_b32_e64 v129, v133\n"
    "v_pk_add_f32 v[128:129], v[128:129], v[132:133]\n"
    "s_nop 1\n"
    "v_add_f32_dpp v128, v128, v128 row_ror:8 row_mask:0xf bank_mask:0xf\n"
    "v_add_f32_dpp v129, v129, v129 row_ror:8 row_mask:0xf bank_mask:0xf\n"
    "s_waitcnt lgkmcnt(0)\n"
    "v_pk_fma_f32 v[92:93], v[96:97], v[128:129], v[92:93]\n"
    "v_cvt_pk_bf16_f32 v92, v92, v93\n"
    "s_mov_b32 exec_lo, 0xff\n"
    "s_mov_b32 exec_hi, 0xff\n"
    "buffer_store_dword v92, v62, s[32:35], s3 offen sc1\n"
    "s_mov_b64 exec, -1\n"
    ".Lk_Lw0:\n"
    "s_cmp_eq_u64 s[66:67], 0\n"
    "s_cbranch_scc1 .Lk_Lw1\n"
    "buffer_load_dwordx4 v[128:131], v8, s[20:23], 0 offen offset:128 sc1\n"
    "buffer_load_dwordx4 v[132:135], v8, s[20:23], s62 offen offset:128 sc1\n"
    "buffer_load_dwordx4 v[136:139], v8, s[20:23], s63 offen offset:128 sc1\n"
    "buffer_load_dwordx4 v[140:143], v9, s[20:23], 0 offen offset:128 sc1\n"
    "s_ff1_i32_b64 s59, s[66:67]\n"
    "v_readlane_b32 s58, v88, s59\n"
    "ds_read_b64 v[92:93], v63 offset:11776\n"
    "s_lshl_b32 s3, s52, 5\n"
    "s_add_u32 s3, s3, s59\n"
    "s_lshl2_add_u32 s3, s3, s53\n"
    "s_lshl_b32 s3, s3, 8\n"
    "s_waitcnt lgkmcnt(1)\n"
    "v_mul_f32 v96, s58, v98\n"
    "v_mul_f32 v97, s58, v99\n"
    "s_waitcnt vmcnt(3)\n"
    "v_xor_b32 v128, v52, v128\n"
    "v_xor_b32 v129, v53, v129\n"
    "v_xor_b32 v130, v54, v130\n"
    "v_xor_b32 v131, v55, v131\n"
    "v_or_b32_e64 v14, v128, v129\n"
    "v_or3_b32 v14, v14, v130, v131\n"
    "s_waitcnt vmcnt(2)\n"
    "v_xor_b32 v132, v52, v132\n"
    "v_xor_b32 v133, v53, v133\n"
    "v_xor_b32 v134, v54, v134\n"
    "v_xor_b32_e64 v135, v55, v135\n"
    "v_or3_b32 v14, v14, v132, v133\n"
    "v_or3_b32 v14, v14, v134, v135\n"
    "s_waitcnt vmcnt(1)\n"
    "v_xor_b32 v136, v52, v136\n"
    "v_xor_b32 v137, v53, v137\n"
    "v_xor_b32 v138, v54, v138\n"
    "v_xor_b32_e64 v139, v55, v139\n"
    "v_or3_b32 v14, v14, v136, v137\n"
    "v_or3_b32 v14, v14, v138, v139\n"
    "s_waitcnt vmcnt(0)\n"
    "v_xor_b32 v140, v52, v140\n"
    "v_xor_b32 v141, v53, v141\n"
    "v_xor_b32 v142, v54, v142\n"
    "v_xor_b32 v143, v55, v143\n"
    "v_and_b32 v140, v89, v140\n"
    "v_and_b32 v141, v89, v141\n"
    "v_and_b32 v142, v89, v142\n"
    "v_and_b32_e64 v143, v89, v143\n"
    "v_or3_b32 v14, v14, v140, v141\n"
    "v_or3_b32 v14, v14, v142, v143\n"
    "v_and_b32 v14, 0xff, v14\n"
    "v_cmp_ne_u32 vcc, 0, v14\n"
    "s_cbranch_vccnz .Lk_R_f1\n"
    ".Lk_B_f1:\n"
    "v_pk_add_f32 v[128:129], v[128:129], v[132:133]\n"
    "v_pk_add_f32 v[130:131], v[130:131], v[134:135]\n"
    "v_pk_add_f32 v[136:137], v[136:137], v[140:141]\n"
    "v_pk_add_f32 v[138:139], v[138:139], v[142:143]\n"
    "v_pk_add_f32 v[128:129], v[128:129], v[136:137]\n"
    "v_pk_add_f32 v[130:131], v[130:131], v[138:139]\n"
    "s_nop 1\n"
    "v_permlane32_swap_b32 v128, v130\n"
    "v_permlane32_swap_b32_e64 v129, v131\n"
    "v_pk_add_f32 v[132:133], v[128:129], v[130:131]\n"
    "v_pk_add_f32 v[128:129], v[128:129], v[130:131]\n"
    "s_nop 1\n"
    "v_permlane16_swap_b32 v128, v132\n"
    "v_permlane16_swap_b32_e64 v129, v133\n"
    "v_pk_add_f32 v[128:129], v[128:129], v[132:133]\n"
    "s_nop 1\n"
    "v_add_f32_dpp v128, v128, v128 row_ror:8 row_mask:0xf bank_mask:0xf\n"
    "v_add_f32_dpp v129, v129, v129 row_ror:8 row_mask:0xf bank_mask:0xf\n"
    "s_waitcnt lgkmcnt(0)\n"
    "v_pk_fma_f32 v[92:93], v[96:97], v[128:129], v[92:93]\n"
    "v_cvt_pk_bf16_f32 v92, v92, v93\n"
    "s_mov_b32 exec_lo, 0xff\n"
    "s_mov_b32 exec_hi, 0xff\n"
    "buffer_store_dword v92, v62, s[32:35], s3 offen sc1\n"
    "s_mov_b64 exec, -1\n"
    ".Lk_Lw1:\n"
    ".Lk_Lexitn:\n"
    "s_endpgm\n"
    ".Lk_LroleA:\n"
    "v_and_b32 v1, 63, v0\n"
    "v_and_b32 v2, 31, v0\n"
    "v_bfe_u32 v12, v0, 5, 1\n"
    "v_lshlrev_b32_e64 v13, 4, v1\n"
    "s_add_u32 s54, s56, s57\n"
    "s_and_b32 s58, s57, 7\n"
    "s_cselect_b32 s54, 99, s54\n"
    "s_cmp_lt_u32 s54, 28\n"
    "s_cselect_b32 s54, s54, -1\n"
    "s_mov_b32 s11, 0xffffff00\n"
    "s_lshl_b32 s98, s48, 12\n"
    "v_lshlrev_b32 v3, 4, v2\n"
    "v_bfe_u32 v16, v1, 3, 2\n"
    "v_xor_b32 v18, s48, v16\n"
    "v_lshlrev_b32 v18, 5, v18\n"
    "v_bfe_u32 v16, v1, 2, 2\n"
    "v_lshl_add_u32 v18, v16, 7, v18\n"
    "v_lshrrev_b32_e64 v16, 4, v1\n"
    "v_lshl_add_u32 v18, v16, 10, v18\n"
    "v_add_u32 v18, 81920, v18\n"
    "v_and_b32_e64 v16, 3, v1\n"
    "v_lshl_add_u32 v7, v16, 3, v18\n"
    "v_bfrev_b32 v16, v16\n"
    "v_lshrrev_b32 v16, 27, v16\n"
    "v_add_u32_e64 v8, v16, v18\n"
    "v_bfe_u32 v16, v1, 1, 3\n"
    "v_xor_b32_e64 v16, s66, v16\n"
    "v_mul_u32_u24 v16, 2064, v16\n"
    "v_and_b32_e64 v17, 1, v1\n"
    "v_lshl_add_u32 v16, v17, 7, v16\n"
    "v_lshl_add_u32 v16, v12, 4, v16\n"
    "v_add_u32 v16, 15872, v16\n"
    "v_bfe_u32 v17, v1, 4, 1\n"
    "v_lshl_add_u32 v11, v17, 8, v16\n"
    "v_mul_u32_u24 v17, 16512, v17\n"
    "v_add_u32_e64 v9, v17, v16\n"
    "s_mul_i32 s58, s48, 16512\n"
    "s_add_u32 m0, s58, 15872\n"
    "v_and_b32 v10, 60, v1\n"
    "s_mov_b64 s[16:17], s[20:21]\n"
    "s_mov_b32 s62, 0xffff0000\n"
    "s_mov_b32 s63, 0xffff0000\n"
    "s_lshl_b32 s10, s49, 6\n"
    "s_add_u32 s10, s10, s50\n"
    "s_lshl_b32 s10, s10, 4\n"
    "s_add_u32 s10, s10, 458752\n"
    "s_cmp_eq_u32 s48, 3\n"
    "s_cselect_b32 s64, 0xff, 0\n"
    "s_lshl2_add_u32 s59, s50, s48\n"
    "s_lshl_b32 s60, s59, 6\n"
    "s_lshl1_add_u32 s60, s49, s60\n"
    "s_lshl_b32 s60, s60, 11\n"
    "s_add_u32 s68, s60, 4194304\n"
    "s_lshr_b32 s60, s60, 4\n"
    "s_add_u32 s9, s60, 3144704\n"
    "s_lshl_b32 s60, s49, 6\n"
    "s_add_u32 s60, s60, s59\n"
    "s_lshl_b32 s60, s60, 10\n"
    "s_add_u32 s69, s60, 1048576\n"
    "v_lshlrev_b32 v4, 2, v1\n"
    "s_barrier\n"
    "v_lshlrev_b32 v16, 2, v12\n"
    "v_sub_u32_e64 v16, v2, v16\n"
    "v_cvt_f32_i32 v17, v16\n"
    "v_mul_f32 v17, 0x72000000, v17\n"
    "s_mov_b32 s58, 0xf1800000\n"
    "v_subrev_f32 v128, 0xf1800000, v17\n"
    "v_subrev_f32 v129, 0x71800000, v17\n"
    "v_subrev_f32 v130, 0x72400000, v17\n"
    "v_subrev_f32 v131, 0x72a00000, v17\n"
    "v_subrev_f32 v132, 0x73700000, v17\n"
    "v_subrev_f32 v133, 0x73880000, v17\n"
    "v_subrev_f32 v134, 0x73980000, v17\n"
    "v_subrev_f32 v135, 0x73a80000, v17\n"
    "v_subrev_f32 v136, 0x73f80000, v17\n"
    "v_subrev_f32 v137, 0x74040000, v17\n"
    "v_subrev_f32 v138, 0x740c0000, v17\n"
    "v_subrev_f32 v139, 0x74140000, v17\n"
    "v_subrev_f32 v140, 0x743c0000, v17\n"
    "v_subrev_f32 v141, 0x74440000, v17\n"
    "v_subrev_f32 v142, 0x744c0000, v17\n"
    "v_subrev_f32 v143, 0x74540000, v17\n"
    "v_med3_f32 v128, v128, s58, 0\n"
    "v_med3_f32 v129, v129, s58, 0\n"
    "v_med3_f32 v130, v130, s58, 0\n"
    "v_med3_f32 v131, v131, s58, 0\n"
    "v_med3_f32 v132, v132, s58, 0\n"
    "v_med3_f32 v133, v133, s58, 0\n"
    "v_med3_f32 v134, v134, s58, 0\n"
    "v_med3_f32 v135, v135, s58, 0\n"
    "v_med3_f32 v136, v136, s58, 0\n"
    "v_med3_f32 v137, v137, s58, 0\n"
    "v_med3_f32 v138, v138, s58, 0\n"
    "v_med3_f32 v139, v139, s58, 0\n"
    "v_med3_f32 v140, v140, s58, 0\n"
    "v_med3_f32 v141, v141, s58, 0\n"
    "v_med3_f32 v142, v142, s58, 0\n"
    "v_med3_f32 v143, v143, s58, 0\n"
    "v_and_b32 v17, 16, v1\n"
    "v_sub_u32 v16, v16, v17\n"
    "v_and_b32 v17, 1, v16\n"
    "v_lshlrev_b32 v17, 4, v17\n"
    "v_mov_b32 v18, 0x3f80\n"
    "v_lshlrev_b32 v17, v17, v18\n"
    "v_ashrrev_i32 v16, 1, v16\n"
    "v_cmp_eq_u32 vcc, 0, v16\n"
    "s_nop 1\n"
    "v_cndmask_b32 v144, 0, v17, vcc\n"
    "v_cmp_eq_u32 vcc, 1, v16\n"
    "s_nop 1\n"
    "v_cndmask_b32 v145, 0, v17, vcc\n"
    "v_cmp_eq_u32 vcc, 4, v16\n"
    "s_nop 1\n"
    "v_cndmask_b32 v146, 0, v17, vcc\n"
    "v_cmp_eq_u32 vcc, 5, v16\n"
    "s_nop 1\n"
    "v_cndmask_b32 v147, 0, v17, vcc\n"
    "ds_write_b128 v13, v[128:131] offset:5120\n"
    "ds_write_b128 v13, v[132:135] offset:6144\n"
    "ds_write_b128 v13, v[136:139] offset:7168\n"
    "ds_write_b128 v13, v[140:143] offset:8192\n"
    "ds_write_b128 v13, v[144:147] offset:9216\n"
    "s_mov_b32 s58, 0x3f803f80\n"
    "s_xor_b32 s65, s67, 12288\n"
    "buffer_load_dwordx4 v[88:91], v14, s[12:15], s65 offen\n"
    "s_xor_b32 s65, s67, 14336\n"
    "buffer_load_dwordx4 v[92:95], v14, s[12:15], s65 offen\n"
    "s_xor_b32 s65, s67, 512\n"
    "buffer_load_dwordx4 v[96:99], v14, s[12:15], s65 offen\n"
    "s_xor_b32 s65, s67, 2560\n"
    "buffer_load_dwordx4 v[100:103], v14, s[12:15], s65 offen\n"
    "s_xor_b32 s65, s67, 4608\n"
    "buffer_load_dwordx4 v[104:107], v14, s[12:15], s65 offen\n"
    "s_xor_b32 s65, s67, 6656\n"
    "buffer_load_dwordx4 v[108:111], v14, s[12:15], s65 offen\n"
    "s_xor_b32 s65, s67, 8704\n"
    "buffer_load_dwordx4 v[112:115], v14, s[12:15], s65 offen\n"
    "s_xor_b32 s65, s67, 10752\n"
    "buffer_load_dwordx4 v[116:119], v14, s[12:15], s65 offen\n"
    "s_xor_b32 s65, s67, 12800\n"
    "buffer_load_dwordx4 v[120:123], v14, s[12:15], s65 offen\n"
    "s_xor_b32 s65, s67, 14848\n"
    "buffer_load_dwordx4 v[124:127], v14, s[12:15], s65 offen\n"
    "v_mov_b32 v15, 1\n"
    "s_mov_b64 exec, 1\n"
    "ds_add_u32 v15, v15 offset:13255\n"
    "s_mov_b64 exec, -1\n"
    "s_setprio 1\n"
    "s_waitcnt vmcnt(16)\n"
    "v_dot2_f32_bf16 v16, v64, s58, 0\n"
    "v_dot2_f32_bf16 v24, v64, v64, 0\n"
    "v_lshlrev_b32 v128, 16, v64\n"
    "v_and_b32 v129, s62, v64\n"
    "s_waitcnt vmcnt(15)\n"
    "v_dot2_f32_bf16 v17, v68, s58, 0\n"
    "v_dot2_f32_bf16 v25, v68, v68, 0\n"
    "v_lshlrev_b32 v130, 16, v68\n"
    "v_and_b32 v131, s62, v68\n"
    "v_dot2c_f32_bf16 v16, s58, v65\n"
    "v_dot2c_f32_bf16 v24, v65, v65\n"
    "v_lshlrev_b32 v144, 16, v65\n"
    "v_and_b32 v145, s62, v65\n"
    "v_dot2c_f32_bf16 v17, s58, v69\n"
    "v_dot2c_f32_bf16 v25, v69, v69\n"
    "v_lshlrev_b32 v146, 16, v69\n"
    "v_and_b32 v147, s62, v69\n"
    "v_dot2c_f32_bf16 v16, s58, v66\n"
    "v_dot2c_f32_bf16 v24, v66, v66\n"
    "v_lshlrev_b32 v160, 16, v66\n"
    "v_and_b32 v161, s62, v66\n"
    "v_dot2c_f32_bf16 v17, s58, v70\n"
    "v_dot2c_f32_bf16 v25, v70, v70\n"
    "v_lshlrev_b32 v162, 16, v70\n"
    "v_and_b32 v163, s62, v70\n"
    "v_dot2c_f32_bf16 v16, s58, v67\n"
    "v_dot2c_f32_bf16 v24, v67, v67\n"
    "v_lshlrev_b32 v176, 16, v67\n"
    "v_and_b32 v177, s62, v67\n"
    "v_dot2c_f32_bf16 v17, s58, v71\n"
    "v_dot2c_f32_bf16 v25, v71, v71\n"
    "v_lshlrev_b32 v178, 16, v71\n"
    "v_and_b32_e64 v179, s62, v71\n"
    "s_waitcnt vmcnt(14)\n"
    "v_dot2_f32_bf16 v18, v72, s58, 0\n"
    "v_dot2_f32_bf16 v26, v72, v72, 0\n"
    "v_lshlrev_b32 v132, 16, v72\n"
    "v_and_b32_e64 v133, s62, v72\n"
    "s_waitcnt vmcnt(13)\n"
    "v_dot2_f32_bf16 v19, v76, s58, 0\n"
    "v_dot2_f32_bf16 v27, v76, v76, 0\n"
    "v_lshlrev_b32 v134, 16, v76\n"
    "v_and_b32 v135, s62, v76\n"
    "v_dot2c_f32_bf16 v18, s58, v73\n"
    "v_dot2c_f32_bf16 v26, v73, v73\n"
    "v_lshlrev_b32 v148, 16, v73\n"
    "v_and_b32 v149, s62, v73\n"
    "v_dot2c_f32_bf16 v19, s58, v77\n"
    "v_dot2c_f32_bf16 v27, v77, v77\n"
    "v_lshlrev_b32 v150, 16, v77\n"
    "v_and_b32 v151, s62, v77\n"
    "v_dot2c_f32_bf16 v18, s58, v74\n"
    "v_dot2c_f32_bf16 v26, v74, v74\n"
    "v_lshlrev_b32 v164, 16, v74\n"
    "v_and_b32 v165, s62, v74\n"
    "v_dot2c_f32_bf16 v19, s58, v78\n"
    "v_dot2c_f32_bf16 v27, v78, v78\n"
    "v_lshlrev_b32 v166, 16, v78\n"
    "v_and_b32 v167, s62, v78\n"
    "v_dot2c_f32_bf16 v18, s58, v75\n"
    "v_dot2c_f32_bf16 v26, v75, v75\n"
    "v_lshlrev_b32 v180, 16, v75\n"
    "v_and_b32 v181, s62, v75\n"
    "v_dot2c_f32_bf16 v19, s58, v79\n"
    "v_dot2c_f32_bf16 v27, v79, v79\n"
    "v_lshlrev_b32 v182, 16, v79\n"
    "v_and_b32_e64 v183, s62, v79\n"
    "s_waitcnt vmcnt(12)\n"
    "v_dot2_f32_bf16 v20, v80, s58, 0\n"
    "v_dot2_f32_bf16 v28, v80, v80, 0\n"
    "v_lshlrev_b32 v136, 16, v80\n"
    "v_and_b32_e64 v137, s62, v80\n"
    "s_waitcnt vmcnt(11)\n"
    "v_dot2_f32_bf16 v21, v84, s58, 0\n"
    "v_dot2_f32_bf16 v29, v84, v84, 0\n"
    "v_lshlrev_b32 v138, 16, v84\n"
    "v_and_b32 v139, s62, v84\n"
    "v_dot2c_f32_bf16 v20, s58, v81\n"
    "v_dot2c_f32_bf16 v28, v81, v81\n"
    "v_lshlrev_b32 v152, 16, v81\n"
    "v_and_b32 v153, s62, v81\n"
    "v_dot2c_f32_bf16 v21, s58, v85\n"
    "v_dot2c_f32_bf16 v29, v85, v85\n"
    "v_lshlrev_b32 v154, 16, v85\n"
    "v_and_b32 v155, s62, v85\n"
    "v_dot2c_f32_bf16 v20, s58, v82\n"
    "v_dot2c_f32_bf16 v28, v82, v82\n"
    "v_lshlrev_b32 v168, 16, v82\n"
    "v_and_b32 v169, s62, v82\n"
    "v_dot2c_f32_bf16 v21, s58, v86\n"
    "v_dot2c_f32_bf16 v29, v86, v86\n"
    "v_lshlrev_b32 v170, 16, v86\n"
    "v_and_b32 v171, s62, v86\n"
    "v_dot2c_f32_bf16 v20, s58, v83\n"
    "v_dot2c_f32_bf16 v28, v83, v83\n"
    "v_lshlrev_b32 v184, 16, v83\n"
    "v_and_b32 v185, s62, v83\n"
    "v_dot2c_f32_bf16 v21, s58, v87\n"
    "v_dot2c_f32_bf16 v29, v87, v87\n"
    "v_lshlrev_b32 v186, 16, v87\n"
    "v_and_b32_e64 v187, s62, v87\n"
    "s_waitcnt vmcnt(9)\n"
    "v_dot2_f32_bf16 v22, v88, s58, 0\n"
    "v_dot2_f32_bf16 v30, v88, v88, 0\n"
    "v_lshlrev_b32 v140, 16, v88\n"
    "v_and_b32_e64 v141, s62, v88\n"
    "s_waitcnt vmcnt(8)\n"
    "v_dot2_f32_bf16 v23, v92, s58, 0\n"
    "v_dot2_f32_bf16 v31, v92, v92, 0\n"
    "v_lshlrev_b32 v142, 16, v92\n"
    "v_and_b32 v143, s62, v92\n"
    "v_dot2c_f32_bf16 v22, s58, v89\n"
    "v_dot2c_f32_bf16 v30, v89, v89\n"
    "v_lshlrev_b32 v156, 16, v89\n"
    "v_and_b32 v157, s62, v89\n"
    "v_dot2c_f32_bf16 v23, s58, v93\n"
    "v_dot2c_f32_bf16 v31, v93, v93\n"
    "v_lshlrev_b32 v158, 16, v93\n"
    "v_and_b32 v159, s62, v93\n"
    "v_dot2c_f32_bf16 v22, s58, v90\n"
    "v_dot2c_f32_bf16 v30, v90, v90\n"
    "v_lshlrev_b32 v172, 16, v90\n"
    "v_and_b32 v173, s62, v90\n"
    "v_dot2c_f32_bf16 v23, s58, v94\n"
    "v_dot2c_f32_bf16 v31, v94, v94\n"
    "v_lshlrev_b32 v174, 16, v94\n"
    "v_and_b32 v175, s62, v94\n"
    "v_dot2c_f32_bf16 v22, s58, v91\n"
    "v_dot2c_f32_bf16 v30, v91, v91\n"
    "v_lshlrev_b32 v188, 16, v91\n"
    "v_and_b32 v189, s62, v91\n"
    "v_dot2c_f32_bf16 v23, s58, v95\n"
    "v_dot2c_f32_bf16 v31, v95, v95\n"
    "v_lshlrev_b32 v190, 16, v95\n"
    "v_and_b32_e64 v191, s62, v95\n"
    "v_readfirstlane_b32 s55, v6\n"
    "s_nop 0\n"
    "s_add_u32 s55, s55, 1\n"
    "s_mul_i32 s8, s55, 0x9e3779b1\n"
    "s_waitcnt vmcnt(7)\n"
    "v_dot2c_f32_bf16 v16, s58, v96\n"
    "v_dot2c_f32_bf16 v24, v96, v96\n"
    "v_lshlrev_b32 v192, 16, v96\n"
    "v_and_b32 v193, s62, v96\n"
    "s_waitcnt vmcnt(6)\n"
    "v_dot2c_f32_bf16 v17, s58, v100\n"
    "v_dot2c_f32_bf16 v25, v100, v100\n"
    "v_lshlrev_b32 v194, 16, v100\n"
    "v_and_b32 v195, s62, v100\n"
    "v_dot2c_f32_bf16 v16, s58, v97\n"
    "v_dot2c_f32_bf16 v24, v97, v97\n"
    "v_lshlrev_b32 v208, 16, v97\n"
    "v_and_b32 v209, s62, v97\n"
    "v_dot2c_f32_bf16 v17, s58, v101\n"
    "v_dot2c_f32_bf16 v25, v101, v101\n"
    "v_lshlrev_b32 v210, 16, v101\n"
    "v_and_b32 v211, s62, v101\n"
    "v_dot2c_f32_bf16 v16, s58, v98\n"
    "v_dot2c_f32_bf16 v24, v98, v98\n"
    "v_lshlrev_b32 v224, 16, v98\n"
    "v_and_b32 v225, s62, v98\n"
    "v_dot2c_f32_bf16 v17, s58, v102\n"
    "v_dot2c_f32_bf16 v25, v102, v102\n"
    "v_lshlrev_b32 v226, 16, v102\n"
    "v_and_b32 v227, s62, v102\n"
    "v_dot2c_f32_bf16 v16, s58, v99\n"
    "v_dot2c_f32_bf16 v24, v99, v99\n"
    "v_lshlrev_b32 v240, 16, v99\n"
    "v_and_b32 v241, s62, v99\n"
    "v_dot2c_f32_bf16 v17, s58, v103\n"
    "v_dot2c_f32_bf16 v25, v103, v103\n"
    "v_lshlrev_b32 v242, 16, v103\n"
    "v_and_b32 v243, s62, v103\n"
    "s_waitcnt vmcnt(5)\n"
    "v_dot2c_f32_bf16 v18, s58, v104\n"
    "v_dot2c_f32_bf16 v26, v104, v104\n"
    "v_lshlrev_b32 v196, 16, v104\n"
    "v_and_b32 v197, s62, v104\n"
    "s_waitcnt vmcnt(4)\n"
    "v_dot2c_f32_bf16 v19, s58, v108\n"
    "v_dot2c_f32_bf16 v27, v108, v108\n"
    "v_lshlrev_b32 v198, 16, v108\n"
    "v_and_b32 v199, s62, v108\n"
    "v_dot2c_f32_bf16 v18, s58, v105\n"
    "v_dot2c_f32_bf16 v26, v105, v105\n"
    "v_lshlrev_b32 v212, 16, v105\n"
    "v_and_b32 v213, s62, v105\n"
    "v_dot2c_f32_bf16 v19, s58, v109\n"
    "v_dot2c_f32_bf16 v27, v109, v109\n"
    "v_lshlrev_b32 v214, 16, v109\n"
    "v_and_b32 v215, s62, v109\n"
    "v_dot2c_f32_bf16 v18, s58, v106\n"
    "v_dot2c_f32_bf16 v26, v106, v106\n"
    "v_lshlrev_b32 v228, 16, v106\n"
    "v_and_b32 v229, s62, v106\n"
    "v_dot2c_f32_bf16 v19, s58, v110\n"
    "v_dot2c_f32_bf16 v27, v110, v110\n"
    "v_lshlrev_b32 v230, 16, v110\n"
    "v_and_b32 v231, s62, v110\n"
    "v_dot2c_f32_bf16 v18, s58, v107\n"
    "v_dot2c_f32_bf16 v26, v107, v107\n"
    "v_lshlrev_b32 v244, 16, v107\n"
    "v_and_b32 v245, s62, v107\n"
    "v_dot2c_f32_bf16 v19, s58, v111\n"
    "v_dot2c_f32_bf16 v27, v111, v111\n"
    "v_lshlrev_b32 v246, 16, v111\n"
    "v_and_b32 v247, s62, v111\n"
    "s_waitcnt vmcnt(3)\n"
    "v_dot2c_f32_bf16 v20, s58, v112\n"
    "v_dot2c_f32_bf16 v28, v112, v112\n"
    "v_lshlrev_b32 v200, 16, v112\n"
    "v_and_b32 v201, s62, v112\n"
    "s_waitcnt vmcnt(2)\n"
    "v_dot2c_f32_bf16 v21, s58, v116\n"
    "v_dot2c_f32_bf16 v29, v116, v116\n"
    "v_lshlrev_b32 v202, 16, v116\n"
    "v_and_b32 v203, s62, v116\n"
    "v_dot2c_f32_bf16 v20, s58, v113\n"
    "v_dot2c_f32_bf16 v28, v113, v113\n"
    "v_lshlrev_b32 v216, 16, v113\n"
    "v_and_b32 v217, s62, v113\n"
    "v_dot2c_f32_bf16 v21, s58, v117\n"
    "v_dot2c_f32_bf16 v29, v117, v117\n"
    "v_lshlrev_b32 v218, 16, v117\n"
    "v_and_b32 v219, s62, v117\n"
    "v_dot2c_f32_bf16 v20, s58, v114\n"
    "v_dot2c_f32_bf16 v28, v114, v114\n"
    "v_lshlrev_b32 v232, 16, v114\n"
    "v_and_b32 v233, s62, v114\n"
    "v_dot2c_f32_bf16 v21, s58, v118\n"
    "v_dot2c_f32_bf16 v29, v118, v118\n"
    "v_lshlrev_b32 v234, 16, v118\n"
    "v_and_b32 v235, s62, v118\n"
    "v_dot2c_f32_bf16 v20, s58, v115\n"
    "v_dot2c_f32_bf16 v28, v115, v115\n"
    "v_lshlrev_b32 v248, 16, v115\n"
    "v_and_b32 v249, s62, v115\n"
    "v_dot2c_f32_bf16 v21, s58, v119\n"
    "v_dot2c_f32_bf16 v29, v119, v119\n"
    "v_lshlrev_b32 v250, 16, v119\n"
    "v_and_b32 v251, s62, v119\n"
    "s_waitcnt vmcnt(1)\n"
    "v_dot2c_f32_bf16 v22, s58, v120\n"
    "v_dot2c_f32_bf16 v30, v120, v120\n"
    "v_lshlrev_b32 v204, 16, v120\n"
    "v_and_b32 v205, s62, v120\n"
    "s_waitcnt vmcnt(0)\n"
    "v_dot2c_f32_bf16 v23, s58, v124\n"
    "v_dot2c_f32_bf16 v31, v124, v124\n"
    "v_lshlrev_b32 v206, 16, v124\n"
    "v_and_b32 v207, s62, v124\n"
    "v_dot2c_f32_bf16 v22, s58, v121\n"
    "v_dot2c_f32_bf16 v30, v121, v121\n"
    "v_lshlrev_b32 v220, 16, v121\n"
    "v_and_b32 v221, s62, v121\n"
    "v_dot2c_f32_bf16 v23, s58, v125\n"
    "v_dot2c_f32_bf16 v31, v125, v125\n"
    "v_lshlrev_b32 v222, 16, v125\n"
    "v_and_b32 v223, s62, v125\n"
    "v_dot2c_f32_bf16 v22, s58, v122\n"
    "v_dot2c_f32_bf16 v30, v122, v122\n"
    "v_lshlrev_b32 v236, 16, v122\n"
    "v_and_b32 v237, s62, v122\n"
    "v_dot2c_f32_bf16 v23, s58, v126\n"
    "v_dot2c_f32_bf16 v31, v126, v126\n"
    "v_lshlrev_b32 v238, 16, v126\n"
    "v_and_b32 v239, s62, v126\n"
    "v_dot2c_f32_bf16 v22, s58, v123\n"
    "v_dot2c_f32_bf16 v30, v123, v123\n"
    "v_lshlrev_b32 v252, 16, v123\n"
    "v_and_b32 v253, s62, v123\n"
    "v_dot2c_f32_bf16 v23, s58, v127\n"
    "v_dot2c_f32_bf16 v31, v127, v127\n"
    "v_lshlrev_b32 v254, 16, v127\n"
    "v_and_b32 v255, s62, v127\n"
    "v_permlane16_swap_b32 v16, v20\n"
    "v_permlane16_swap_b32 v17, v21\n"
    "v_permlane16_swap_b32 v18, v22\n"
    "v_permlane16_swap_b32 v19, v23\n"
    "v_permlane16_swap_b32 v24, v28\n"
    "v_permlane16_swap_b32 v25, v29\n"
    "v_permlane16_swap_b32 v26, v30\n"
    "v_permlane16_swap_b32 v27, v31\n"
    "v_pk_add_f32 v[16:17], v[16:17], v[20:21]\n"
    "v_pk_add_f32 v[18:19], v[18:19], v[22:23]\n"
    "v_pk_add_f32 v[24:25], v[24:25], v[28:29]\n"
    "v_pk_add_f32 v[26:27], v[26:27], v[30:31]\n"
    "v_add_f32_dpp v16, v16, v16 quad_perm:[1,0,3,2] row_mask:0xf bank_mask:0xf\n"
    "v_add_f32_dpp v17, v17, v17 quad_perm:[1,0,3,2] row_mask:0xf bank_mask:0xf\n"
    "v_add_f32_dpp v18, v18, v18 quad_perm:[1,0,3,2] row_mask:0xf bank_mask:0xf\n"
    "v_add_f32_dpp v19, v19, v19 quad_perm:[1,0,3,2] row_mask:0xf bank_mask:0xf\n"
    "v_add_f32_dpp v24, v24, v24 quad_perm:[1,0,3,2] row_mask:0xf bank_mask:0xf\n"
    "v_add_f32_dpp v25, v25, v25 quad_perm:[1,0,3,2] row_mask:0xf bank_mask:0xf\n"
    "v_add_f32_dpp v26, v26, v26 quad_perm:[1,0,3,2] row_mask:0xf bank_mask:0xf\n"
    "v_add_f32_dpp v27, v27, v27 quad_perm:[1,0,3,2] row_mask:0xf bank_mask:0xf\n"
    "v_add_f32_dpp v16, v16, v16 quad_perm:[2,3,0,1] row_mask:0xf bank_mask:0xf\n"
    "v_add_f32_dpp v17, v17, v17 quad_perm:[2,3,0,1] row_mask:0xf bank_mask:0xf\n"
    "v_add_f32_dpp v18, v18, v18 quad_perm:[2,3,0,1] row_mask:0xf bank_mask:0xf\n"
    "v_add_f32_dpp v19, v19, v19 quad_perm:[2,3,0,1] row_mask:0xf bank_mask:0xf\n"
    "v_add_f32_dpp v24, v24, v24 quad_perm:[2,3,0,1] row_mask:0xf bank_mask:0xf\n"
    "v_add_f32_dpp v25, v25, v25 quad_perm:[2,3,0,1] row_mask:0xf bank_mask:0xf\n"
    "v_add_f32_dpp v26, v26, v26 quad_perm:[2,3,0,1] row_mask:0xf bank_mask:0xf\n"
    "v_add_f32_dpp v27, v27, v27 quad_perm:[2,3,0,1] row_mask:0xf bank_mask:0xf\n"
    "v_add_f32_dpp v16, v16, v16 row_half_mirror row_mask:0xf bank_mask:0xf\n"
    "v_add_f32_dpp v17, v17, v17 row_half_mirror row_mask:0xf bank_mask:0xf\n"
    "v_add_f32_dpp v18, v18, v18 row_half_mirror row_mask:0xf bank_mask:0xf\n"
    "v_add_f32_dpp v19, v19, v19 row_half_mirror row_mask:0xf bank_mask:0xf\n"
    "v_add_f32_dpp v24, v24, v24 row_half_mirror row_mask:0xf bank_mask:0xf\n"
    "v_add_f32_dpp v25, v25, v25 row_half_mirror row_mask:0xf bank_mask:0xf\n"
    "v_add_f32_dpp v26, v26, v26 row_half_mirror row_mask:0xf bank_mask:0xf\n"
    "v_add_f32_dpp v27, v27, v27 row_half_mirror row_mask:0xf bank_mask:0xf\n"
    "v_add_f32_dpp v16, v16, v16 row_mirror row_mask:0xf bank_mask:0xf\n"
    "v_add_f32_dpp v17, v17, v17 row_mirror row_mask:0xf bank_mask:0xf\n"
    "v_add_f32_dpp v18, v18, v18 row_mirror row_mask:0xf bank_mask:0xf\n"
    "v_add_f32_dpp v19, v19, v19 row_mirror row_mask:0xf bank_mask:0xf\n"
    "v_add_f32_dpp v24, v24, v24 row_mirror row_mask:0xf bank_mask:0xf\n"
    "v_add_f32_dpp v25, v25, v25 row_mirror row_mask:0xf bank_mask:0xf\n"
    "v_add_f32_dpp v26, v26, v26 row_mirror row_mask:0xf bank_mask:0xf\n"
    "v_add_f32_dpp v27, v27, v27 row_mirror row_mask:0xf bank_mask:0xf\n"
    "s_mov_b32 s60, 0x3b000000\n"
    "v_pk_mul_f32 v[16:17], v[16:17], s[60:61] op_sel_hi:[1,0]\n"
    "v_pk_mul_f32 v[18:19], v[18:19], s[60:61] op_sel_hi:[1,0]\n"
    "v_pk_mul_f32 v[24:25], v[24:25], s[60:61] op_sel_hi:[1,0]\n"
    "v_pk_mul_f32 v[26:27], v[26:27], s[60:61] op_sel_hi:[1,0]\n"
    "v_pk_fma_f32 v[24:25], v[16:17], v[16:17], v[24:25] neg_lo:[1,0,0] neg_hi:[1,0,0]\n"
    "v_pk_fma_f32 v[26:27], v[18:19], v[18:19], v[26:27] neg_lo:[1,0,0] neg_hi:[1,0,0]\n"
    "v_max_f32 v24, 0, v24\n"
    "v_max_f32 v25, 0, v25\n"
    "v_max_f32 v26, 0, v26\n"
    "v_max_f32 v27, 0, v27\n"
    "v_cvt_pk_bf16_f32 v24, 0, v24\n"
    "v_cvt_pk_bf16_f32 v25, 0, v25\n"
    "v_cvt_pk_bf16_f32 v26, 0, v26\n"
    "v_cvt_pk_bf16_f32 v27, 0, v27\n"
    "v_cvt_pk_bf16_f32 v16, 0, v16\n"
    "v_cvt_pk_bf16_f32 v17, 0, v17\n"
    "v_cvt_pk_bf16_f32 v18, 0, v18\n"
    "v_cvt_pk_bf16_f32 v19, 0, v19\n"
    "v_add_f32 v24, 0x3727c5ac, v24\n"
    "v_add_f32 v25, 0x3727c5ac, v25\n"
    "v_add_f32 v26, 0x3727c5ac, v26\n"
    "v_add_f32 v27, 0x3727c5ac, v27\n"
    "v_cvt_pk_bf16_f32 v24, 0, v24\n"
    "v_cvt_pk_bf16_f32 v25, 0, v25\n"
    "v_cvt_pk_bf16_f32 v26, 0, v26\n"
    "v_cvt_pk_bf16_f32 v27, 0, v27\n"
    "v_sqrt_f32 v24, v24\n"
    "v_sqrt_f32 v25, v25\n"
    "v_sqrt_f32 v26, v26\n"
    "v_sqrt_f32 v27, v27\n"
    "v_cvt_pk_bf16_f32 v24, 0, v24\n"
    "v_cvt_pk_bf16_f32 v25, 0, v25\n"
    "v_cvt_pk_bf16_f32 v26, 0, v26\n"
    "v_cvt_pk_bf16_f32 v27, 0, v27\n"
    "v_rcp_f32 v32, v24\n"
    "v_rcp_f32 v34, v25\n"
    "v_rcp_f32 v36, v26\n"
    "v_rcp_f32 v38, v27\n"
    "v_mul_f32 v33, -v16, v32\n"
    "v_mul_f32 v35, -v17, v34\n"
    "v_mul_f32 v37, -v18, v36\n"
    "v_mul_f32 v39, -v19, v38\n"
    "v_mov_b64 v[40:41], v[32:33]\n"
    "v_mov_b64 v[42:43], v[34:35]\n"
    "v_mov_b64 v[44:45], v[36:37]\n"
    "v_mov_b64 v[46:47], v[38:39]\n"
    "v_permlane16_swap_b32 v32, v40\n"
    "v_permlane16_swap_b32 v33, v41\n"
    "v_permlane16_swap_b32 v34, v42\n"
    "v_permlane16_swap_b32 v35, v43\n"
    "v_permlane16_swap_b32 v36, v44\n"
    "v_permlane16_swap_b32 v37, v45\n"
    "v_permlane16_swap_b32 v38, v46\n"
    "v_permlane16_swap_b32 v39, v47\n"
    "v_pk_fma_f32 v[128:129], v[128:129], v[32:33], v[32:33] op_sel:[0,0,1] op_sel_hi:[1,0,1]\n"
    "v_pk_fma_f32 v[130:131], v[130:131], v[34:35], v[34:35] op_sel:[0,0,1] op_sel_hi:[1,0,1]\n"
    "v_pk_fma_f32 v[132:133], v[132:133], v[36:37], v[36:37] op_sel:[0,0,1] op_sel_hi:[1,0,1]\n"
    "v_pk_fma_f32 v[134:135], v[134:135], v[38:39], v[38:39] op_sel:[0,0,1] op_sel_hi:[1,0,1]\n"
    "v_pk_fma_f32 v[136:137], v[136:137], v[40:41], v[40:41] op_sel:[0,0,1] op_sel_hi:[1,0,1]\n"
    "v_pk_fma_f32 v[138:139], v[138:139], v[42:43], v[42:43] op_sel:[0,0,1] op_sel_hi:[1,0,1]\n"
    "v_pk_fma_f32 v[140:141], v[140:141], v[44:45], v[44:45] op_sel:[0,0,1] op_sel_hi:[1,0,1]\n"
    "v_pk_fma_f32 v[142:143], v[142:143], v[46:47], v[46:47] op_sel:[0,0,1] op_sel_hi:[1,0,1]\n"
    "v_pk_fma_f32 v[144:145], v[144:145], v[32:33], v[32:33] op_sel:[0,0,1] op_sel_hi:[1,0,1]\n"
    "v_pk_fma_f32 v[146:147], v[146:147], v[34:35], v[34:35] op_sel:[0,0,1] op_sel_hi:[1,0,1]\n"
    "v_pk_fma_f32 v[148:149], v[148:149], v[36:37], v[36:37] op_sel:[0,0,1] op_sel_hi:[1,0,1]\n"
    "v_pk_fma_f32 v[150:151], v[150:151], v[38:39], v[38:39] op_sel:[0,0,1] op_sel_hi:[1,0,1]\n"
    "v_pk_fma_f32 v[152:153], v[152:153], v[40:41], v[40:41] op_sel:[0,0,1] op_sel_hi:[1,0,1]\n"
    "v_pk_fma_f32 v[154:155], v[154:155], v[42:43], v[42:43] op_sel:[0,0,1] op_sel_hi:[1,0,1]\n"
    "v_pk_fma_f32 v[156:157], v[156:157], v[44:45], v[44:45] op_sel:[0,0,1] op_sel_hi:[1,0,1]\n"
    "v_pk_fma_f32 v[158:159], v[158:159], v[46:47], v[46:47] op_sel:[0,0,1] op_sel_hi:[1,0,1]\n"
    "v_pk_fma_f32 v[160:161], v[160:161], v[32:33], v[32:33] op_sel:[0,0,1] op_sel_hi:[1,0,1]\n"
    "v_pk_fma_f32 v[162:163], v[162:163], v[34:35], v[34:35] op_sel:[0,0,1] op_sel_hi:[1,0,1]\n"
    "v_pk_fma_f32 v[164:165], v[164:165], v[36:37], v[36:37] op_sel:[0,0,1] op_sel_hi:[1,0,1]\n"
    "v_pk_fma_f32 v[166:167], v[166:167], v[38:39], v[38:39] op_sel:[0,0,1] op_sel_hi:[1,0,1]\n"
    "v_pk_fma_f32 v[168:169], v[168:169], v[40:41], v[40:41] op_sel:[0,0,1] op_sel_hi:[1,0,1]\n"
    "v_pk_fma_f32 v[170:171], v[170:171], v[42:43], v[42:43] op_sel:[0,0,1] op_sel_hi:[1,0,1]\n"
    "v_pk_fma_f32 v[172:173], v[172:173], v[44:45], v[44:45] op_sel:[0,0,1] op_sel_hi:[1,0,1]\n"
    "v_pk_fma_f32 v[174:175], v[174:175], v[46:47], v[46:47] op_sel:[0,0,1] op_sel_hi:[1,0,1]\n"
    "v_pk_fma_f32 v[176:177], v[176:177], v[32:33], v[32:33] op_sel:[0,0,1] op_sel_hi:[1,0,1]\n"
    "v_pk_fma_f32 v[178:179], v[178:179], v[34:35], v[34:35] op_sel:[0,0,1] op_sel_hi:[1,0,1]\n"
    "v_pk_fma_f32 v[180:181], v[180:181], v[36:37], v[36:37] op_sel:[0,0,1] op_sel_hi:[1,0,1]\n"
    "v_pk_fma_f32 v[182:183], v[182:183], v[38:39], v[38:39] op_sel:[0,0,1] op_sel_hi:[1,0,1]\n"
    "v_pk_fma_f32 v[184:185], v[184:185], v[40:41], v[40:41] op_sel:[0,0,1] op_sel_hi:[1,0,1]\n"
    "v_pk_fma_f32 v[186:187], v[186:187], v[42:43], v[42:43] op_sel:[0,0,1] op_sel_hi:[1,0,1]\n"
    "v_pk_fma_f32 v[188:189], v[188:189], v[44:45], v[44:45] op_sel:[0,0,1] op_sel_hi:[1,0,1]\n"
    "v_pk_fma_f32 v[190:191], v[190:191], v[46:47], v[46:47] op_sel:[0,0,1] op_sel_hi:[1,0,1]\n"
    "v_pk_fma_f32 v[192:193], v[192:193], v[32:33], v[32:33] op_sel:[0,0,1] op_sel_hi:[1,0,1]\n"
    "v_pk_fma_f32 v[194:195], v[194:195], v[34:35], v[34:35] op_sel:[0,0,1] op_sel_hi:[1,0,1]\n"
    "v_pk_fma_f32 v[196:197], v[196:197], v[36:37], v[36:37] op_sel:[0,0,1] op_sel_hi:[1,0,1]\n"
    "v_pk_fma_f32 v[198:199], v[198:199], v[38:39], v[38:39] op_sel:[0,0,1] op_sel_hi:[1,0,1]\n"
    "v_pk_fma_f32 v[200:201], v[200:201], v[40:41], v[40:41] op_sel:[0,0,1] op_sel_hi:[1,0,1]\n"
    "v_pk_fma_f32 v[202:203], v[202:203], v[42:43], v[42:43] op_sel:[0,0,1] op_sel_hi:[1,0,1]\n"
    "v_pk_fma_f32 v[204:205], v[204:205], v[44:45], v[44:45] op_sel:[0,0,1] op_sel_hi:[1,0,1]\n"
    "v_pk_fma_f32 v[206:207], v[206:207], v[46:47], v[46:47] op_sel:[0,0,1] op_sel_hi:[1,0,1]\n"
    "v_pk_fma_f32 v[208:209], v[208:209], v[32:33], v[32:33] op_sel:[0,0,1] op_sel_hi:[1,0,1]\n"
    "v_pk_fma_f32 v[210:211], v[210:211], v[34:35], v[34:35] op_sel:[0,0,1] op_sel_hi:[1,0,1]\n"
    "v_pk_fma_f32 v[212:213], v[212:213], v[36:37], v[36:37] op_sel:[0,0,1] op_sel_hi:[1,0,1]\n"
    "v_pk_fma_f32 v[214:215], v[214:215], v[38:39], v[38:39] op_sel:[0,0,1] op_sel_hi:[1,0,1]\n"
    "v_pk_fma_f32 v[216:217], v[216:217], v[40:41], v[40:41] op_sel:[0,0,1] op_sel_hi:[1,0,1]\n"
    "v_pk_fma_f32 v[218:219], v[218:219], v[42:43], v[42:43] op_sel:[0,0,1] op_sel_hi:[1,0,1]\n"
    "v_pk_fma_f32 v[220:221], v[220:221], v[44:45], v[44:45] op_sel:[0,0,1] op_sel_hi:[1,0,1]\n"
    "v_pk_fma_f32 v[222:223], v[222:223], v[46:47], v[46:47] op_sel:[0,0,1] op_sel_hi:[1,0,1]\n"
    "v_pk_fma_f32 v[224:225], v[224:225], v[32:33], v[32:33] op_sel:[0,0,1] op_sel_hi:[1,0,1]\n"
    "v_pk_fma_f32 v[226:227], v[226:227], v[34:35], v[34:35] op_sel:[0,0,1] op_sel_hi:[1,0,1]\n"
    "v_pk_fma_f32 v[228:229], v[228:229], v[36:37], v[36:37] op_sel:[0,0,1] op_sel_hi:[1,0,1]\n"
    "v_pk_fma_f32 v[230:231], v[230:231], v[38:39], v[38:39] op_sel:[0,0,1] op_sel_hi:[1,0,1]\n"
    "v_pk_fma_f32 v[232:233], v[232:233], v[40:41], v[40:41] op_sel:[0,0,1] op_sel_hi:[1,0,1]\n"
    "v_pk_fma_f32 v[234:235], v[234:235], v[42:43], v[42:43] op_sel:[0,0,1] op_sel_hi:[1,0,1]\n"
    "v_pk_fma_f32 v[236:237], v[236:237], v[44:45], v[44:45] op_sel:[0,0,1] op_sel_hi:[1,0,1]\n"
    "v_pk_fma_f32 v[238:239], v[238:239], v[46:47], v[46:47] op_sel:[0,0,1] op_sel_hi:[1,0,1]\n"
    "v_pk_fma_f32 v[240:241], v[240:241], v[32:33], v[32:33] op_sel:[0,0,1] op_sel_hi:[1,0,1]\n"
    "v_pk_fma_f32 v[242:243], v[242:243], v[34:35], v[34:35] op_sel:[0,0,1] op_sel_hi:[1,0,1]\n"
    "v_pk_fma_f32 v[244:245], v[244:245], v[36:37], v[36:37] op_sel:[0,0,1] op_sel_hi:[1,0,1]\n"
    "v_pk_fma_f32 v[246:247], v[246:247], v[38:39], v[38:39] op_sel:[0,0,1] op_sel_hi:[1,0,1]\n"
    "v_pk_fma_f32 v[248:249], v[248:249], v[40:41], v[40:41] op_sel:[0,0,1] op_sel_hi:[1,0,1]\n"
    "v_pk_fma_f32 v[250:251], v[250:251], v[42:43], v[42:43] op_sel:[0,0,1] op_sel_hi:[1,0,1]\n"
    "v_pk_fma_f32 v[252:253], v[252:253], v[44:45], v[44:45] op_sel:[0,0,1] op_sel_hi:[1,0,1]\n"
    "v_pk_fma_f32 v[254:255], v[254:255], v[46:47], v[46:47] op_sel:[0,0,1] op_sel_hi:[1,0,1]\n"
    "s_waitcnt lgkmcnt(0)\n"
    "s_barrier\n"
    "ds_read_b128 v[16:19], v3 offset:2048\n"
    "ds_read_b128 v[24:27], v3 offset:0\n"
    "ds_read_b128 v[20:23], v3 offset:2560\n"
    "ds_read_b128 v[28:31], v3 offset:512\n"
    "s_waitcnt lgkmcnt(2)\n"
    "s_nop 0\n"
    "v_pk_fma_f32 v[128:129], v[128:129], v[16:17], v[24:25]\n"
    "v_pk_fma_f32 v[130:131], v[130:131], v[16:17], v[24:25]\n"
    "v_pk_fma_f32 v[132:133], v[132:133], v[16:17], v[24:25]\n"
    "v_pk_fma_f32 v[134:135], v[134:135], v[16:17], v[24:25]\n"
    "v_cvt_pk_bf16_f32 v128, v128, v129\n"
    "v_cvt_pk_bf16_f32 v130, v130, v131\n"
    "v_cvt_pk_bf16_f32 v132, v132, v133\n"
    "v_cvt_pk_bf16_f32 v134, v134, v135\n"
    "ds_read_b128 v[32:35], v3 offset:3072\n"
    "ds_read_b128 v[40:43], v3 offset:1024\n"
    "ds_read_b128 v[36:39], v3 offset:3584\n"
    "ds_read_b128 v[44:47], v3 offset:1536\n"
    "ds_write_addtid_b32 v128 offset:0\n"
    "ds_write_addtid_b32 v130 offset:2064\n"
    "ds_write_addtid_b32 v132 offset:4128\n"
    "ds_write_addtid_b32 v134 offset:6192\n"
    "v_pk_fma_f32 v[136:137], v[136:137], v[16:17], v[24:25]\n"
    "v_pk_fma_f32 v[138:139], v[138:139], v[16:17], v[24:25]\n"
    "v_pk_fma_f32 v[140:141], v[140:141], v[16:17], v[24:25]\n"
    "v_pk_fma_f32 v[142:143], v[142:143], v[16:17], v[24:25]\n"
    "v_cvt_pk_bf16_f32 v136, v136, v137\n"
    "v_cvt_pk_bf16_f32 v138, v138, v139\n"
    "v_cvt_pk_bf16_f32 v140, v140, v141\n"
    "v_cvt_pk_bf16_f32 v142, v142, v143\n"
    "ds_write_addtid_b32 v136 offset:8256\n"
    "ds_write_addtid_b32 v138 offset:10320\n"
    "ds_write_addtid_b32 v140 offset:12384\n"
    "ds_write_addtid_b32 v142 offset:14448\n"
    "v_pk_fma_f32 v[144:145], v[144:145], v[18:19], v[26:27]\n"
    "v_pk_fma_f32 v[146:147], v[146:147], v[18:19], v[26:27]\n"
    "v_pk_fma_f32 v[148:149], v[148:149], v[18:19], v[26:27]\n"
    "v_pk_fma_f32 v[150:151], v[150:151], v[18:19], v[26:27]\n"
    "v_cvt_pk_bf16_f32 v144, v144, v145\n"
    "v_cvt_pk_bf16_f32 v146, v146, v147\n"
    "v_cvt_pk_bf16_f32 v148, v148, v149\n"
    "v_cvt_pk_bf16_f32 v150, v150, v151\n"
    "ds_write_addtid_b32 v144 offset:256\n"
    "ds_write_addtid_b32 v146 offset:2320\n"
    "ds_write_addtid_b32 v148 offset:4384\n"
    "ds_write_addtid_b32 v150 offset:6448\n"
    "v_pk_fma_f32 v[152:153], v[152:153], v[18:19], v[26:27]\n"
    "v_pk_fma_f32 v[154:155], v[154:155], v[18:19], v[26:27]\n"
    "v_pk_fma_f32 v[156:157], v[156:157], v[18:19], v[26:27]\n"
    "v_pk_fma_f32 v[158:159], v[158:159], v[18:19], v[26:27]\n"
    "v_cvt_pk_bf16_f32 v152, v152, v153\n"
    "v_cvt_pk_bf16_f32 v154, v154, v155\n"
    "v_cvt_pk_bf16_f32 v156, v156, v157\n"
    "v_cvt_pk_bf16_f32 v158, v158, v159\n"
    "ds_write_addtid_b32 v152 offset:8512\n"
    "ds_write_addtid_b32 v154 offset:10576\n"
    "ds_write_addtid_b32 v156 offset:12640\n"
    "ds_write_addtid_b32 v158 offset:14704\n"
    "s_waitcnt lgkmcnt(15)\n"
    "s_nop 0\n"
    "v_pk_fma_f32 v[160:161], v[160:161], v[20:21], v[28:29]\n"
    "v_pk_fma_f32 v[162:163], v[162:163], v[20:21], v[28:29]\n"
    "v_pk_fma_f32 v[164:165], v[164:165], v[20:21], v[28:29]\n"
    "v_pk_fma_f32 v[166:167], v[166:167], v[20:21], v[28:29]\n"
    "v_cvt_pk_bf16_f32 v160, v160, v161\n"
    "v_cvt_pk_bf16_f32 v162, v162, v163\n"
    "v_cvt_pk_bf16_f32 v164, v164, v165\n"
    "v_cvt_pk_bf16_f32 v166, v166, v167\n"
    "ds_write_addtid_b32 v160 offset:512\n"
    "ds_write_addtid_b32 v162 offset:2576\n"
    "ds_write_addtid_b32 v164 offset:4640\n"
    "ds_write_addtid_b32 v166 offset:6704\n"
    "v_pk_fma_f32 v[168:169], v[168:169], v[20:21], v[28:29]\n"
    "v_pk_fma_f32 v[170:171], v[170:171], v[20:21], v[28:29]\n"
    "v_pk_fma_f32 v[172:173], v[172:173], v[20:21], v[28:29]\n"
    "v_pk_fma_f32 v[174:175], v[174:175], v[20:21], v[28:29]\n"
    "v_cvt_pk_bf16_f32 v168, v168, v169\n"
    "v_cvt_pk_bf16_f32 v170, v170, v171\n"
    "v_cvt_pk_bf16_f32 v172, v172, v173\n"
    "v_cvt_pk_bf16_f32 v174, v174, v175\n"
    "ds_write_addtid_b32 v168 offset:8768\n"
    "ds_write_addtid_b32 v170 offset:10832\n"
    "ds_write_addtid_b32 v172 offset:12896\n"
    "ds_write_addtid_b32 v174 offset:14960\n"
    "v_pk_fma_f32 v[176:177], v[176:177], v[22:23], v[30:31]\n"
    "v_pk_fma_f32 v[178:179], v[178:179], v[22:23], v[30:31]\n"
    "v_pk_fma_f32 v[180:181], v[180:181], v[22:23], v[30:31]\n"
    "v_pk_fma_f32 v[182:183], v[182:183], v[22:23], v[30:31]\n"
    "v_cvt_pk_bf16_f32 v176, v176, v177\n"
    "v_cvt_pk_bf16_f32 v178, v178, v179\n"
    "v_cvt_pk_bf16_f32 v180, v180, v181\n"
    "v_cvt_pk_bf16_f32 v182, v182, v183\n"
    "ds_write_addtid_b32 v176 offset:768\n"
    "ds_write_addtid_b32 v178 offset:2832\n"
    "ds_write_addtid_b32 v180 offset:4896\n"
    "ds_write_addtid_b32 v182 offset:6960\n"
    "v_pk_fma_f32 v[184:185], v[184:185], v[22:23], v[30:31]\n"
    "v_pk_fma_f32 v[186:187], v[186:187], v[22:23], v[30:31]\n"
    "v_pk_fma_f32 v[188:189], v[188:189], v[22:23], v[30:31]\n"
    "v_pk_fma_f32 v[190:191], v[190:191], v[22:23], v[30:31]\n"
    "v_cvt_pk_bf16_f32 v184, v184, v185\n"
    "v_cvt_pk_bf16_f32 v186, v186, v187\n"
    "v_cvt_pk_bf16_f32 v188, v188, v189\n"
    "v_cvt_pk_bf16_f32 v190, v190, v191\n"
    "ds_write_addtid_b32 v184 offset:9024\n"
    "ds_write_addtid_b32 v186 offset:11088\n"
    "ds_write_addtid_b32 v188 offset:13152\n"
    "ds_write_addtid_b32 v190 offset:15216\n"
    "s_waitcnt lgkmcnt(15)\n"
    "s_nop 0\n"
    "v_pk_fma_f32 v[192:193], v[192:193], v[32:33], v[40:41]\n"
    "v_pk_fma_f32 v[194:195], v[194:195], v[32:33], v[40:41]\n"
    "v_pk_fma_f32 v[196:197], v[196:197], v[32:33], v[40:41]\n"
    "v_pk_fma_f32 v[198:199], v[198:199], v[32:33], v[40:41]\n"
    "v_cvt_pk_bf16_f32 v192, v192, v193\n"
    "v_cvt_pk_bf16_f32 v194, v194, v195\n"
    "v_cvt_pk_bf16_f32 v196, v196, v197\n"
    "v_cvt_pk_bf16_f32 v198, v198, v199\n"
    "ds_write_addtid_b32 v192 offset:1024\n"
    "ds_write_addtid_b32 v194 offset:3088\n"
    "ds_write_addtid_b32 v196 offset:5152\n"
    "ds_write_addtid_b32 v198 offset:7216\n"
    "v_pk_fma_f32 v[200:201], v[200:201], v[32:33], v[40:41]\n"
    "v_pk_fma_f32 v[202:203], v[202:203], v[32:33], v[40:41]\n"
    "v_pk_fma_f32 v[204:205], v[204:205], v[32:33], v[40:41]\n"
    "v_pk_fma_f32 v[206:207], v[206:207], v[32:33], v[40:41]\n"
    "v_cvt_pk_bf16_f32 v200, v200, v201\n"
    "v_cvt_pk_bf16_f32 v202, v202, v203\n"
    "v_cvt_pk_bf16_f32 v204, v204, v205\n"
    "v_cvt_pk_bf16_f32 v206, v206, v207\n"
    "ds_write_addtid_b32 v200 offset:9280\n"
    "ds_write_addtid_b32 v202 offset:11344\n"
    "ds_write_addtid_b32 v204 offset:13408\n"
    "ds_write_addtid_b32 v206 offset:15472\n"
    "v_pk_fma_f32 v[208:209], v[208:209], v[34:35], v[42:43]\n"
    "v_pk_fma_f32 v[210:211], v[210:211], v[34:35], v[42:43]\n"
    "v_pk_fma_f32 v[212:213], v[212:213], v[34:35], v[42:43]\n"
    "v_pk_fma_f32 v[214:215], v[214:215], v[34:35], v[42:43]\n"
    "v_cvt_pk_bf16_f32 v208, v208, v209\n"
    "v_cvt_pk_bf16_f32 v210, v210, v211\n"
    "v_cvt_pk_bf16_f32 v212, v212, v213\n"
    "v_cvt_pk_bf16_f32 v214, v214, v215\n"
    "ds_write_addtid_b32 v208 offset:1280\n"
    "ds_write_addtid_b32 v210 offset:3344\n"
    "ds_write_addtid_b32 v212 offset:5408\n"
    "ds_write_addtid_b32 v214 offset:7472\n"
    "v_pk_fma_f32 v[216:217], v[216:217], v[34:35], v[42:43]\n"
    "v_pk_fma_f32 v[218:219], v[218:219], v[34:35], v[42:43]\n"
    "v_pk_fma_f32 v[220:221], v[220:221], v[34:35], v[42:43]\n"
    "v_pk_fma_f32 v[222:223], v[222:223], v[34:35], v[42:43]\n"
    "v_cvt_pk_bf16_f32 v216, v216, v217\n"
    "v_cvt_pk_bf16_f32 v218, v218, v219\n"
    "v_cvt_pk_bf16_f32 v220, v220, v221\n"
    "v_cvt_pk_bf16_f32 v222, v222, v223\n"
    "ds_read_b64_tr_b16 v[48:49], v7 offset:0\n"
    "ds_read_b64_tr_b16 v[50:51], v7 offset:512\n"
    "ds_read_b64_tr_b16 v[52:53], v7 offset:4096\n"
    "ds_read_b64_tr_b16 v[54:55], v7 offset:4608\n"
    "ds_read_b64_tr_b16 v[56:57], v7 offset:8192\n"
    "ds_read_b64_tr_b16 v[58:59], v7 offset:8704\n"
    "ds_read_b64_tr_b16 v[60:61], v7 offset:12288\n"
    "ds_read_b64_tr_b16 v[62:63], v7 offset:12800\n"
    "ds_write_addtid_b32 v216 offset:9536\n"
    "ds_write_addtid_b32 v218 offset:11600\n"
    "ds_write_addtid_b32 v220 offset:13664\n"
    "ds_write_addtid_b32 v222 offset:15728\n"
    "s_waitcnt lgkmcnt(15)\n"
    "s_nop 0\n"
    "v_pk_fma_f32 v[224:225], v[224:225], v[36:37], v[44:45]\n"
    "v_pk_fma_f32 v[226:227], v[226:227], v[36:37], v[44:45]\n"
    "v_pk_fma_f32 v[228:229], v[228:229], v[36:37], v[44:45]\n"
    "v_pk_fma_f32 v[230:231], v[230:231], v[36:37], v[44:45]\n"
    "v_pk_fma_f32 v[232:233], v[232:233], v[36:37], v[44:45]\n"
    "v_pk_fma_f32 v[234:235], v[234:235], v[36:37], v[44:45]\n"
    "v_cvt_pk_bf16_f32 v224, v224, v225\n"
    "v_cvt_pk_bf16_f32 v226, v226, v227\n"
    "v_cvt_pk_bf16_f32 v228, v228, v229\n"
    "v_cvt_pk_bf16_f32 v230, v230, v231\n"
    "v_cvt_pk_bf16_f32 v232, v232, v233\n"
    "v_cvt_pk_bf16_f32 v234, v234, v235\n"
    "s_waitcnt lgkmcnt(0)\n"
    "s_barrier\n"
    "ds_read_b128 v[96:99], v9 offset:0\n"
    "ds_read_b128 v[100:103], v9 offset:33024\n"
    "ds_read_b128 v[104:107], v9 offset:32\n"
    "ds_read_b128 v[108:111], v9 offset:33056\n"
    "ds_write_addtid_b32 v224 offset:1536\n"
    "ds_write_addtid_b32 v226 offset:3600\n"
    "ds_write_addtid_b32 v228 offset:5664\n"
    "ds_write_addtid_b32 v230 offset:7728\n"
    "ds_write_addtid_b32 v232 offset:9792\n"
    "s_waitcnt lgkmcnt(7)\n"
    "s_nop 0\n"
    "v_mfma_f32_32x32x16_bf16 v[64:79], v[48:51], v[96:99], 0\n"
    "ds_read_b128 v[112:115], v9 offset:64\n"
    "ds_read_b64_tr_b16 v[128:129], v7 offset:16384\n"
    "ds_read_b64_tr_b16 v[130:131], v7 offset:16896\n"
    "ds_write_addtid_b32 v234 offset:11856\n"
    "v_fma_f32 v236, v236, v36, v44\n"
    "v_fma_f32 v237, v237, v37, v45\n"
    "v_cvt_pk_bf16_f32 v236, v236, v237\n"
    "v_mfma_f32_32x32x16_bf16 v[80:95], v[48:51], v[100:103], 0\n"
    "ds_read_b128 v[116:119], v9 offset:33088\n"
    "ds_read_b64_tr_b16 v[132:133], v7 offset:20480\n"
    "ds_write_addtid_b32 v236 offset:13920\n"
    "v_fma_f32 v238, v238, v36, v44\n"
    "v_fma_f32 v239, v239, v37, v45\n"
    "v_cvt_pk_bf16_f32 v238, v238, v239\n"
    "s_waitcnt lgkmcnt(12)\n"
    "s_nop 0\n"
    "v_mfma_f32_32x32x16_bf16 v[64:79], v[52:55], v[104:107], v[64:79]\n"
    "ds_read_b128 v[120:123], v9 offset:96\n"
    "ds_read_b64_tr_b16 v[134:135], v7 offset:20992\n"
    "ds_write_addtid_b32 v238 offset:15984\n"
    "v_fma_f32 v240, v240, v38, v46\n"
    "v_fma_f32 v241, v241, v39, v47\n"
    "v_cvt_pk_bf16_f32 v240, v240, v241\n"
    "v_mfma_f32_32x32x16_bf16 v[80:95], v[52:55], v[108:111], v[80:95]\n"
    "ds_read_b128 v[124:127], v9 offset:33120\n"
    "ds_read_b64_tr_b16 v[136:137], v7 offset:24576\n"
    "ds_read_b64_tr_b16 v[138:139], v7 offset:25088\n"
    "ds_write_addtid_b32 v240 offset:1792\n"
    "v_fma_f32 v242, v242, v38, v46\n"
    "v_fma_f32 v243, v243, v39, v47\n"
    "v_cvt_pk_bf16_f32 v242, v242, v243\n"
    "s_waitcnt lgkmcnt(9)\n"
    "s_nop 0\n"
    "v_mfma_f32_32x32x16_bf16 v[64:79], v[56:59], v[112:115], v[64:79]\n"
    "ds_read_b128 v[96:99], v9 offset:256\n"
    "ds_read_b64_tr_b16 v[140:141], v7 offset:28672\n"
    "ds_write_addtid_b32 v242 offset:3856\n"
    "v_fma_f32 v244, v244, v38, v46\n"
    "v_fma_f32 v245, v245, v39, v47\n"
    "v_cvt_pk_bf16_f32 v244, v244, v245\n"
    "v_mfma_f32_32x32x16_bf16 v[80:95], v[56:59], v[116:119], v[80:95]\n"
    "ds_read_b128 v[100:103], v9 offset:33280\n"
    "ds_read_b64_tr_b16 v[142:143], v7 offset:29184\n"
    "ds_write_addtid_b32 v244 offset:5920\n"
    "v_fma_f32 v246, v246, v38, v46\n"
    "v_fma_f32 v247, v247, v39, v47\n"
    "v_cvt_pk_bf16_f32 v246, v246, v247\n"
    "s_waitcnt lgkmcnt(9)\n"
    "s_nop 0\n"
    "v_mfma_f32_32x32x16_bf16 v[64:79], v[60:63], v[120:123], v[64:79]\n"
    "ds_read_b128 v[104:107], v9 offset:288\n"
    "ds_read_b64_tr_b16 v[144:145], v7 offset:32768\n"
    "ds_read_b64_tr_b16 v[146:147], v7 offset:33280\n"
    "ds_write_addtid_b32 v246 offset:7984\n"
    "v_fma_f32 v248, v248, v38, v46\n"
    "v_fma_f32 v249, v249, v39, v47\n"
    "v_cvt_pk_bf16_f32 v248, v248, v249\n"
    "v_mfma_f32_32x32x16_bf16 v[80:95], v[60:63], v[124:127], v[80:95]\n"
    "ds_read_b128 v[108:111], v9 offset:33312\n"
    "ds_read_b64_tr_b16 v[148:149], v7 offset:36864\n"
    "ds_write_addtid_b32 v248 offset:10048\n"
    "v_fma_f32 v250, v250, v38, v46\n"
    "v_fma_f32 v251, v251, v39, v47\n"
    "v_cvt_pk_bf16_f32 v250, v250, v251\n"
    "s_waitcnt lgkmcnt(9)\n"
    "s_nop 0\n"
    "v_mfma_f32_32x32x16_bf16 v[64:79], v[128:131], v[96:99], v[64:79]\n"
    "ds_read_b128 v[112:115], v9 offset:320\n"
    "ds_read_b64_tr_b16 v[150:151], v7 offset:37376\n"
    "ds_write_addtid_b32 v250 offset:12112\n"
    "v_fma_f32 v252, v252, v38, v46\n"
    "v_fma_f32 v253, v253, v39, v47\n"
    "v_cvt_pk_bf16_f32 v252, v252, v253\n"
    "v_mfma_f32_32x32x16_bf16 v[80:95], v[128:131], v[100:103], v[80:95]\n"
    "ds_read_b128 v[116:119], v9 offset:33344\n"
    "ds_read_b64_tr_b16 v[152:153], v7 offset:40960\n"
    "ds_read_b64_tr_b16 v[154:155], v7 offset:41472\n"
    "ds_write_addtid_b32 v252 offset:14176\n"
    "v_fma_f32 v254, v254, v38, v46\n"
    "v_fma_f32 v255, v255, v39, v47\n"
    "v_cvt_pk_bf16_f32 v254, v254, v255\n"
    "s_waitcnt lgkmcnt(9)\n"
    "v_mfma_f32_32x32x16_bf16 v[64:79], v[132:135], v[104:107], v[64:79]\n"
    "ds_read_b128 v[120:123], v9 offset:352\n"
    "ds_read_b64_tr_b16 v[156:157], v7 offset:45056\n"
    "ds_write_addtid_b32 v254 offset:16240\n"
    "v_mfma_f32_32x32x16_bf16 v[80:95], v[132:135], v[108:111], v[80:95]\n"
    "ds_read_b128 v[124:127], v9 offset:33376\n"
    "ds_read_b64_tr_b16 v[158:159], v7 offset:45568\n"
    "s_waitcnt lgkmcnt(8)\n"
    "v_mfma_f32_32x32x16_bf16 v[64:79], v[136:139], v[112:115], v[64:79]\n"
    "s_waitcnt lgkmcnt(0)\n"
    "s_barrier\n"
    "ds_read_b128 v[96:99], v9 offset:512\n"
    "ds_read_b64_tr_b16 v[160:161], v7 offset:49152\n"
    "ds_read_b64_tr_b16 v[162:163], v7 offset:49664\n"
    "v_mfma_f32_32x32x16_bf16 v[80:95], v[136:139], v[116:119], v[80:95]\n"
    "ds_read_b128 v[100:103], v9 offset:33536\n"
    "ds_read_b64_tr_b16 v[164:165], v7 offset:53248\n"
    "ds_read_b64_tr_b16 v[166:167], v7 offset:53760\n"
    "v_mfma_f32_32x32x16_bf16 v[64:79], v[140:143], v[120:123], v[64:79]\n"
    "ds_read_b128 v[104:107], v9 offset:544\n"
    "ds_read_b64_tr_b16 v[168:169], v7 offset:57344\n"
    "ds_read_b64_tr_b16 v[170:171], v7 offset:57856\n"
    "v_mfma_f32_32x32x16_bf16 v[80:95], v[140:143], v[124:127], v[80:95]\n"
    "ds_read_b128 v[108:111], v9 offset:33568\n"
    "ds_read_b64_tr_b16 v[172:173], v7 offset:61440\n"
    "ds_read_b64_tr_b16 v[174:175], v7 offset:61952\n"
    "v_add_u32 v6, 65536, v7\n"
    "s_waitcnt lgkmcnt(8)\n"
    "s_nop 0\n"
    "v_mfma_f32_32x32x16_bf16 v[64:79], v[144:147], v[96:99], v[64:79]\n"
    "ds_read_b128 v[112:115], v9 offset:576\n"
    "ds_read_b64_tr_b16 v[176:177], v6 offset:0\n"
    "ds_read_b64_tr_b16 v[178:179], v6 offset:512\n"
    "v_mfma_f32_32x32x16_bf16 v[80:95], v[144:147], v[100:103], v[80:95]\n"
    "ds_read_b128 v[116:119], v9 offset:33600\n"
    "ds_read_b64_tr_b16 v[180:181], v6 offset:4096\n"
    "ds_read_b64_tr_b16 v[182:183], v6 offset:4608\n"
    "s_waitcnt lgkmcnt(8)\n"
    "s_nop 0\n"
    "v_mfma_f32_32x32x16_bf16 v[64:79], v[148:151], v[104:107], v[64:79]\n"
    "ds_read_b128 v[120:123], v9 offset:608\n"
    "ds_read_b64_tr_b16 v[184:185], v6 offset:8192\n"
    "ds_read_b64_tr_b16 v[186:187], v6 offset:8704\n"
    "v_mfma_f32_32x32x16_bf16 v[80:95], v[148:151], v[108:111], v[80:95]\n"
    "ds_read_b128 v[124:127], v9 offset:33632\n"
    "ds_read_b64_tr_b16 v[188:189], v6 offset:12288\n"
    "ds_read_b64_tr_b16 v[190:191], v6 offset:12800\n"
    "s_waitcnt lgkmcnt(8)\n"
    "v_mfma_f32_32x32x16_bf16 v[64:79], v[152:155], v[112:115], v[64:79]\n"
    "ds_read_b128 v[96:99], v9 offset:768\n"
    "v_mfma_f32_32x32x16_bf16 v[80:95], v[152:155], v[116:119], v[80:95]\n"
    "ds_read_b128 v[100:103], v9 offset:33792\n"
    "s_waitcnt lgkmcnt(4)\n"
    "v_mfma_f32_32x32x16_bf16 v[64:79], v[156:159], v[120:123], v[64:79]\n"
    "ds_read_b128 v[104:107], v9 offset:800\n"
    "v_mfma_f32_32x32x16_bf16 v[80:95], v[156:159], v[124:127], v[80:95]\n"
    "ds_read_b128 v[108:111], v9 offset:33824\n"
    "s_waitcnt lgkmcnt(2)\n"
    "v_mfma_f32_32x32x16_bf16 v[64:79], v[160:163], v[96:99], v[64:79]\n"
    "ds_read_b128 v[112:115], v9 offset:832\n"
    "v_mfma_f32_32x32x16_bf16 v[80:95], v[160:163], v[100:103], v[80:95]\n"
    "ds_read_b128 v[116:119], v9 offset:33856\n"
    "s_waitcnt lgkmcnt(2)\n"
    "v_mfma_f32_32x32x16_bf16 v[64:79], v[164:167], v[104:107], v[64:79]\n"
    "ds_read_b128 v[120:123], v9 offset:864\n"
    "v_mfma_f32_32x32x16_bf16 v[80:95], v[164:167], v[108:111], v[80:95]\n"
    "ds_read_b128 v[124:127], v9 offset:33888\n"
    "s_waitcnt lgkmcnt(2)\n"
    "v_mfma_f32_32x32x16_bf16 v[64:79], v[168:171], v[112:115], v[64:79]\n"
    "ds_read_b128 v[96:99], v9 offset:1024\n"
    "v_mfma_f32_32x32x16_bf16 v[80:95], v[168:171], v[116:119], v[80:95]\n"
    "ds_read_b128 v[100:103], v9 offset:34048\n"
    "s_waitcnt lgkmcnt(2)\n"
    "v_mfma_f32_32x32x16_bf16 v[64:79], v[172:175], v[120:123], v[64:79]\n"
    "ds_read_b128 v[104:107], v9 offset:1056\n"
    "v_mfma_f32_32x32x16_bf16 v[80:95], v[172:175], v[124:127], v[80:95]\n"
    "ds_read_b128 v[108:111], v9 offset:34080\n"
    "s_waitcnt lgkmcnt(2)\n"
    "v_mfma_f32_32x32x16_bf16 v[64:79], v[176:179], v[96:99], v[64:79]\n"
    "ds_read_b128 v[112:115], v9 offset:1088\n"
    "v_mfma_f32_32x32x16_bf16 v[80:95], v[176:179], v[100:103], v[80:95]\n"
    "ds_read_b128 v[116:119], v9 offset:34112\n"
    "s_waitcnt lgkmcnt(2)\n"
    "v_mfma_f32_32x32x16_bf16 v[64:79], v[180:183], v[104:107], v[64:79]\n"
    "ds_read_b128 v[120:123], v9 offset:1120\n"
    "v_mfma_f32_32x32x16_bf16 v[80:95], v[180:183], v[108:111], v[80:95]\n"
    "ds_read_b128 v[124:127], v9 offset:34144\n"
    "s_waitcnt lgkmcnt(2)\n"
    "v_mfma_f32_32x32x16_bf16 v[64:79], v[184:187], v[112:115], v[64:79]\n"
    "s_barrier\n"
    "ds_read_b128 v[96:99], v9 offset:1280\n"
    "ds_read_b64_tr_b16 v[192:193], v7 offset:0\n"
    "ds_read_b64_tr_b16 v[194:195], v7 offset:512\n"
    "v_mfma_f32_32x32x16_bf16 v[80:95], v[184:187], v[116:119], v[80:95]\n"
    "ds_read_b128 v[100:103], v9 offset:34304\n"
    "ds_read_b64_tr_b16 v[196:197], v7 offset:4096\n"
    "ds_read_b64_tr_b16 v[198:199], v7 offset:4608\n"
    "s_waitcnt lgkmcnt(6)\n"
    "s_nop 0\n"
    "v_mfma_f32_32x32x16_bf16 v[64:79], v[188:191], v[120:123], v[64:79]\n"
    "ds_read_b128 v[104:107], v9 offset:1312\n"
    "ds_read_b64_tr_b16 v[200:201], v7 offset:8192\n"
    "ds_read_b64_tr_b16 v[202:203], v7 offset:8704\n"
    "v_mfma_f32_32x32x16_bf16 v[80:95], v[188:191], v[124:127], v[80:95]\n"
    "ds_read_b128 v[108:111], v9 offset:34336\n"
    "ds_read_b64_tr_b16 v[204:205], v7 offset:12288\n"
    "ds_read_b64_tr_b16 v[206:207], v7 offset:12800\n"
    "s_waitcnt lgkmcnt(8)\n"
    "v_mfma_f32_32x32x16_bf16 v[64:79], v[192:195], v[96:99], v[64:79]\n"
    "ds_read_b128 v[112:115], v9 offset:1344\n"
    "v_mfma_f32_32x32x16_bf16 v[80:95], v[192:195], v[100:103], v[80:95]\n"
    "ds_read_b128 v[116:119], v9 offset:34368\n"
    "s_waitcnt lgkmcnt(4)\n"
    "v_mfma_f32_32x32x16_bf16 v[64:79], v[196:199], v[104:107], v[64:79]\n"
    "ds_read_b128 v[120:123], v9 offset:1376\n"
    "v_mfma_f32_32x32x16_bf16 v[80:95], v[196:199], v[108:111], v[80:95]\n"
    "ds_read_b128 v[124:127], v9 offset:34400\n"
    "s_waitcnt lgkmcnt(2)\n"
    "v_mfma_f32_32x32x16_bf16 v[64:79], v[200:203], v[112:115], v[64:79]\n"
    "s_barrier\n"
    "ds_read_b128 v[96:99], v9 offset:1536\n"
    "ds_read_b64_tr_b16 v[208:209], v7 offset:16384\n"
    "ds_read_b64_tr_b16 v[210:211], v7 offset:16896\n"
    "v_mfma_f32_32x32x16_bf16 v[80:95], v[200:203], v[116:119], v[80:95]\n"
    "ds_read_b128 v[100:103], v9 offset:34560\n"
    "ds_read_b64_tr_b16 v[212:213], v7 offset:20480\n"
    "ds_read_b64_tr_b16 v[214:215], v7 offset:20992\n"
    "s_waitcnt lgkmcnt(6)\n"
    "s_nop 0\n"
    "v_mfma_f32_32x32x16_bf16 v[64:79], v[204:207], v[120:123], v[64:79]\n"
    "ds_read_b128 v[104:107], v9 offset:1568\n"
    "ds_read_b64_tr_b16 v[216:217], v7 offset:24576\n"
    "ds_read_b64_tr_b16 v[218:219], v7 offset:25088\n"
    "v_mfma_f32_32x32x16_bf16 v[80:95], v[204:207], v[124:127], v[80:95]\n"
    "ds_read_b128 v[108:111], v9 offset:34592\n"
    "ds_read_b64_tr_b16 v[220:221], v7 offset:28672\n"
    "ds_read_b64_tr_b16 v[222:223], v7 offset:29184\n"
    "s_waitcnt lgkmcnt(8)\n"
    "v_mfma_f32_32x32x16_bf16 v[64:79], v[208:211], v[96:99], v[64:79]\n"
    "ds_read_b128 v[112:115], v9 offset:1600\n"
    "s_mov_b32 s59, 0x3f803f80\n"
    "v_mov_b64 v[36:37], s[58:59]\n"
    "v_mfma_f32_32x32x16_bf16 v[80:95], v[208:211], v[100:103], v[80:95]\n"
    "ds_read_b128 v[116:119], v9 offset:34624\n"
    "v_mov_b64 v[38:39], s[58:59]\n"
    "s_waitcnt lgkmcnt(4)\n"
    "v_mfma_f32_32x32x16_bf16 v[64:79], v[212:215], v[104:107], v[64:79]\n"
    "ds_read_b128 v[120:123], v9 offset:1632\n"
    "v_mov_b64_e64 v[44:45], s[58:59]\n"
    "v_mfma_f32_32x32x16_bf16 v[80:95], v[212:215], v[108:111], v[80:95]\n"
    "ds_read_b128 v[124:127], v9 offset:34656\n"
    "v_mov_b64 v[46:47], s[58:59]\n"
    "s_waitcnt lgkmcnt(2)\n"
    "v_mfma_f32_32x32x16_bf16 v[64:79], v[216:219], v[112:115], v[64:79]\n"
    "s_barrier\n"
    "ds_read_b128 v[96:99], v9 offset:1792\n"
    "ds_read_b64_tr_b16 v[224:225], v7 offset:32768\n"
    "ds_read_b64_tr_b16 v[226:227], v7 offset:33280\n"
    "v_mov_b64 v[164:165], s[58:59]\n"
    "v_mov_b64_e64 v[166:167], s[58:59]\n"
    "v_mfma_f32_32x32x16_bf16 v[80:95], v[216:219], v[116:119], v[80:95]\n"
    "ds_read_b128 v[100:103], v9 offset:1824\n"
    "ds_read_b64_tr_b16 v[228:229], v7 offset:36864\n"
    "ds_read_b64_tr_b16 v[230:231], v7 offset:37376\n"
    "v_mov_b64 v[172:173], s[58:59]\n"
    "s_waitcnt lgkmcnt(6)\n"
    "v_mfma_f32_32x32x16_bf16 v[64:79], v[220:223], v[120:123], v[64:79]\n"
    "ds_read_b128 v[104:107], v9 offset:1856\n"
    "ds_read_b64_tr_b16 v[232:233], v7 offset:40960\n"
    "ds_read_b64_tr_b16 v[234:235], v7 offset:41472\n"
    "v_mov_b64_e64 v[174:175], s[58:59]\n"
    "v_mfma_f32_32x32x16_bf16 v[80:95], v[220:223], v[124:127], v[80:95]\n"
    "ds_read_b128 v[108:111], v9 offset:1888\n"
    "ds_read_b64_tr_b16 v[236:237], v7 offset:45056\n"
    "ds_read_b64_tr_b16 v[238:239], v7 offset:45568\n"
    "v_mov_b64 v[180:181], s[58:59]\n"
    "s_waitcnt lgkmcnt(8)\n"
    "v_mfma_f32_32x32x16_bf16 v[64:79], v[224:227], v[96:99], v[64:79]\n"
    "ds_read_b128 v[112:115], v9 offset:34816\n"
    "v_mov_b64 v[182:183], s[58:59]\n"
    "v_mov_b64_e64 v[188:189], s[58:59]\n"
    "s_waitcnt lgkmcnt(7)\n"
    "v_mfma_f32_32x32x16_bf16 v[64:79], v[228:231], v[100:103], v[64:79]\n"
    "ds_read_b128 v[116:119], v9 offset:34848\n"
    "v_mov_b64 v[190:191], s[58:59]\n"
    "s_waitcnt lgkmcnt(4)\n"
    "v_mfma_f32_32x32x16_bf16 v[64:79], v[232:235], v[104:107], v[64:79]\n"
    "ds_read_b128 v[120:123], v9 offset:34880\n"
    "v_mov_b64 v[196:197], s[58:59]\n"
    "s_waitcnt lgkmcnt(3)\n"
    "v_mfma_f32_32x32x16_bf16 v[64:79], v[236:239], v[108:111], v[64:79]\n"
    "ds_read_b128 v[124:127], v9 offset:34912\n"
    "v_mov_b64 v[198:199], s[58:59]\n"
    "s_waitcnt lgkmcnt(2)\n"
    "v_mfma_f32_32x32x16_bf16 v[80:95], v[224:227], v[112:115], v[80:95]\n"
    "ds_read_b128 v[96:99], v11 offset:0\n"
    "ds_read_b128 v[156:159], v13 offset:9216\n"
    "v_mov_b64_e64 v[204:205], s[58:59]\n"
    "v_mfma_f32_32x32x16_bf16 v[80:95], v[228:231], v[116:119], v[80:95]\n"
    "ds_read_b128 v[100:103], v11 offset:16512\n"
    "v_mov_b64 v[206:207], s[58:59]\n"
    "s_waitcnt lgkmcnt(3)\n"
    "v_mfma_f32_32x32x16_bf16 v[80:95], v[232:235], v[120:123], v[80:95]\n"
    "ds_read_b128 v[104:107], v11 offset:33024\n"
    "v_cvt_pk_bf16_f32 v24, v72, v73\n"
    "v_cvt_pk_bf16_f32 v25, v74, v75\n"
    "v_cvt_pk_bf16_f32 v26, v76, v77\n"
    "v_cvt_pk_bf16_f32 v27, v78, v79\n"
    "v_mfma_f32_32x32x16_bf16 v[80:95], v[236:239], v[124:127], v[80:95]\n"
    "s_barrier\n"
    "ds_read_b128 v[108:111], v11 offset:49536\n"
    "v_cvt_pk_bf16_f32 v16, v64, v65\n"
    "v_cvt_pk_bf16_f32 v17, v66, v67\n"
    "v_cvt_pk_bf16_f32 v18, v68, v69\n"
    "v_cvt_pk_bf16_f32 v19, v70, v71\n"
    "s_waitcnt lgkmcnt(3)\n"
    "v_mfma_f32_32x32x16_bf16 v[64:79], v[24:27], v[156:159], 0\n"
    "buffer_store_dwordx4 v[16:19], v13, s[20:23], s68 offen sc1\n"
    "s_nop 2\n"
    "s_nop 0\n"
    "v_cvt_pk_bf16_f32 v28, v88, v89\n"
    "v_cvt_pk_bf16_f32 v29, v90, v91\n"
    "v_cvt_pk_bf16_f32 v30, v92, v93\n"
    "v_cvt_pk_bf16_f32 v31, v94, v95\n"
    "v_cvt_pk_bf16_f32 v20, v80, v81\n"
    "v_cvt_pk_bf16_f32 v21, v82, v83\n"
    "v_cvt_pk_bf16_f32 v22, v84, v85\n"
    "v_cvt_pk_bf16_f32 v23, v86, v87\n"
    "v_mfma_f32_32x32x16_bf16 v[80:95], v[28:31], v[156:159], 0\n"
    "buffer_store_dwordx4 v[20:23], v13, s[20:23], s68 offen offset:2048 sc1\n"
    "v_add3_u32 v5, s8, v16, v17\n"
    "v_add3_u32 v5, v5, v18, v19\n"
    "v_add3_u32 v5, v5, v20, v21\n"
    "v_add3_u32 v5, v5, v22, v23\n"
    "v_cvt_pk_bf16_f32 v32, v64, v65\n"
    "v_cvt_pk_bf16_f32 v33, v66, v67\n"
    "v_cvt_pk_bf16_f32 v34, v68, v69\n"
    "v_cvt_pk_bf16_f32 v35, v70, v71\n"
    "s_mov_b64 exec, s[62:63]\n"
    "v_cvt_pk_bf16_f32 v32, v72, v73\n"
    "v_cvt_pk_bf16_f32 v33, v74, v75\n"
    "v_cvt_pk_bf16_f32 v34, v76, v77\n"
    "v_cvt_pk_bf16_f32 v35, v78, v79\n"
    "s_mov_b64 exec, -1\n"
    "buffer_store_dwordx4 v[32:35], v13, s[20:23], s68 offen offset:1024 sc1\n"
    "v_add3_u32 v5, v5, v32, v33\n"
    "v_add3_u32 v5, v5, v34, v35\n"
    "v_cvt_pk_bf16_f32 v40, v80, v81\n"
    "v_cvt_pk_bf16_f32 v41, v82, v83\n"
    "v_cvt_pk_bf16_f32 v42, v84, v85\n"
    "v_cvt_pk_bf16_f32 v43, v86, v87\n"
    "s_mov_b64 exec, s[62:63]\n"
    "v_cvt_pk_bf16_f32 v40, v88, v89\n"
    "v_cvt_pk_bf16_f32 v41, v90, v91\n"
    "v_cvt_pk_bf16_f32 v42, v92, v93\n"
    "v_cvt_pk_bf16_f32 v43, v94, v95\n"
    "s_mov_b64 exec, -1\n"
    "buffer_store_dwordx4 v[40:43], v13, s[20:23], s68 offen offset:3072 sc1\n"
    "v_add3_u32 v5, v5, v40, v41\n"
    "v_add3_u32 v5, v5, v42, v43\n"
    "buffer_store_dword v5, v4, s[20:23], s9 offen offset:1024 sc1\n"
    "s_barrier\n"
    "ds_read_b64_tr_b16 v[48:49], v8 offset:49152\n"
    "ds_read_b64_tr_b16 v[50:51], v8 offset:49664\n"
    "ds_read_b64_tr_b16 v[52:53], v8 offset:53248\n"
    "ds_read_b64_tr_b16 v[54:55], v8 offset:53760\n"
    "s_waitcnt lgkmcnt(2)\n"
    "v_mfma_f32_16x16x32_bf16 v[112:115], v[48:51], v[96:99], 0\n"
    "ds_read_b128 v[80:83], v11 offset:32\n"
    "ds_read_b128 v[84:87], v11 offset:16544\n"
    "ds_read_b128 v[88:91], v11 offset:33056\n"
    "ds_read_b64_tr_b16 v[56:57], v8 offset:57344\n"
    "v_mfma_f32_16x16x32_bf16 v[116:119], v[48:51], v[100:103], 0\n"
    "ds_read_b128 v[92:95], v11 offset:49568\n"
    "ds_read_b64_tr_b16 v[58:59], v8 offset:57856\n"
    "v_mfma_f32_16x16x32_bf16 v[120:123], v[48:51], v[104:107], 0\n"
    "ds_read_b128 v[96:99], v11 offset:64\n"
    "ds_read_b64_tr_b16 v[60:61], v8 offset:61440\n"
    "v_mfma_f32_16x16x32_bf16 v[124:127], v[48:51], v[108:111], 0\n"
    "ds_read_b128 v[100:103], v11 offset:16576\n"
    "ds_read_b64_tr_b16 v[62:63], v8 offset:61952\n"
    "s_waitcnt lgkmcnt(8)\n"
    "v_mfma_f32_16x16x32_bf16 v[112:115], v[52:55], v[80:83], v[112:115]\n"
    "ds_read_b128 v[104:107], v11 offset:33088\n"
    "v_add_u32 v6, 65536, v8\n"
    "v_mfma_f32_16x16x32_bf16 v[116:119], v[52:55], v[84:87], v[116:119]\n"
    "ds_read_b128 v[108:111], v11 offset:49600\n"
    "ds_read_b64_tr_b16 v[240:241], v6 offset:0\n"
    "s_waitcnt lgkmcnt(8)\n"
    "v_mfma_f32_16x16x32_bf16 v[120:123], v[52:55], v[88:91], v[120:123]\n"
    "ds_read_b128 v[80:83], v11 offset:96\n"
    "ds_read_b64_tr_b16 v[242:243], v6 offset:512\n"
    "v_mfma_f32_16x16x32_bf16 v[124:127], v[52:55], v[92:95], v[124:127]\n"
    "ds_read_b128 v[84:87], v11 offset:16608\n"
    "ds_read_b64_tr_b16 v[244:245], v6 offset:4096\n"
    "s_waitcnt lgkmcnt(8)\n"
    "v_mfma_f32_16x16x32_bf16 v[112:115], v[56:59], v[96:99], v[112:115]\n"
    "ds_read_b128 v[88:91], v11 offset:33120\n"
    "ds_read_b64_tr_b16 v[246:247], v6 offset:4608\n"
    "v_mfma_f32_16x16x32_bf16 v[116:119], v[56:59], v[100:103], v[116:119]\n"
    "ds_read_b128 v[92:95], v11 offset:49632\n"
    "ds_read_b64_tr_b16 v[248:249], v6 offset:8192\n"
    "s_waitcnt lgkmcnt(9)\n"
    "v_mfma_f32_16x16x32_bf16 v[120:123], v[56:59], v[104:107], v[120:123]\n"
    "ds_read_b128 v[96:99], v11 offset:512\n"
    "ds_read_b64_tr_b16 v[250:251], v6 offset:8704\n"
    "v_mfma_f32_16x16x32_bf16 v[124:127], v[56:59], v[108:111], v[124:127]\n"
    "ds_read_b128 v[100:103], v11 offset:17024\n"
    "ds_read_b64_tr_b16 v[252:253], v6 offset:12288\n"
    "s_waitcnt lgkmcnt(9)\n"
    "v_mfma_f32_16x16x32_bf16 v[112:115], v[60:63], v[80:83], v[112:115]\n"
    "ds_read_b128 v[104:107], v11 offset:33536\n"
    "ds_read_b64_tr_b16 v[254:255], v6 offset:12800\n"
    "v_mfma_f32_16x16x32_bf16 v[116:119], v[60:63], v[84:87], v[116:119]\n"
    "ds_read_b128 v[108:111], v11 offset:50048\n"
    "ds_read_b64_tr_b16 v[224:225], v8 offset:0\n"
    "s_waitcnt lgkmcnt(9)\n"
    "v_mfma_f32_16x16x32_bf16 v[120:123], v[60:63], v[88:91], v[120:123]\n"
    "ds_read_b128 v[80:83], v11 offset:544\n"
    "ds_read_b64_tr_b16 v[226:227], v8 offset:512\n"
    "v_mfma_f32_16x16x32_bf16 v[124:127], v[60:63], v[92:95], v[124:127]\n"
    "ds_read_b128 v[84:87], v11 offset:17056\n"
    "ds_read_b64_tr_b16 v[228:229], v8 offset:4096\n"
    "s_waitcnt lgkmcnt(9)\n"
    "v_mfma_f32_16x16x32_bf16 v[112:115], v[240:243], v[96:99], v[112:115]\n"
    "ds_read_b128 v[88:91], v11 offset:33568\n"
    "ds_read_b64_tr_b16 v[230:231], v8 offset:4608\n"
    "v_mfma_f32_16x16x32_bf16 v[116:119], v[240:243], v[100:103], v[116:119]\n"
    "ds_read_b128 v[92:95], v11 offset:50080\n"
    "ds_read_b64_tr_b16 v[232:233], v8 offset:8192\n"
    "s_waitcnt lgkmcnt(9)\n"
    "v_mfma_f32_16x16x32_bf16 v[120:123], v[240:243], v[104:107], v[120:123]\n"
    "ds_read_b128 v[96:99], v11 offset:576\n"
    "ds_read_b64_tr_b16 v[234:235], v8 offset:8704\n"
    "v_mfma_f32_16x16x32_bf16 v[124:127], v[240:243], v[108:111], v[124:127]\n"
    "ds_read_b128 v[100:103], v11 offset:17088\n"
    "ds_read_b64_tr_b16 v[236:237], v8 offset:12288\n"
    "s_waitcnt lgkmcnt(9)\n"
    "v_mfma_f32_16x16x32_bf16 v[112:115], v[244:247], v[80:83], v[112:115]\n"
    "ds_read_b128 v[104:107], v11 offset:33600\n"
    "ds_read_b64_tr_b16 v[238:239], v8 offset:12800\n"
    "v_mfma_f32_16x16x32_bf16 v[116:119], v[244:247], v[84:87], v[116:119]\n"
    "ds_read_b128 v[108:111], v11 offset:50112\n"
    "ds_read_b64_tr_b16 v[64:65], v8 offset:16384\n"
    "s_waitcnt lgkmcnt(9)\n"
    "v_mfma_f32_16x16x32_bf16 v[120:123], v[244:247], v[88:91], v[120:123]\n"
    "ds_read_b128 v[80:83], v11 offset:608\n"
    "ds_read_b64_tr_b16 v[66:67], v8 offset:16896\n"
    "v_mfma_f32_16x16x32_bf16 v[124:127], v[244:247], v[92:95], v[124:127]\n"
    "ds_read_b128 v[84:87], v11 offset:17120\n"
    "ds_read_b64_tr_b16 v[68:69], v8 offset:20480\n"
    "s_waitcnt lgkmcnt(9)\n"
    "v_mfma_f32_16x16x32_bf16 v[112:115], v[248:251], v[96:99], v[112:115]\n"
    "ds_read_b128 v[88:91], v11 offset:33632\n"
    "ds_read_b64_tr_b16 v[70:71], v8 offset:20992\n"
    "v_mfma_f32_16x16x32_bf16 v[116:119], v[248:251], v[100:103], v[116:119]\n"
    "ds_read_b128 v[92:95], v11 offset:50144\n"
    "ds_read_b64_tr_b16 v[72:73], v8 offset:24576\n"
    "s_waitcnt lgkmcnt(9)\n"
    "v_mfma_f32_16x16x32_bf16 v[120:123], v[248:251], v[104:107], v[120:123]\n"
    "ds_read_b128 v[96:99], v11 offset:1024\n"
    "ds_read_b64_tr_b16 v[74:75], v8 offset:25088\n"
    "v_mfma_f32_16x16x32_bf16 v[124:127], v[248:251], v[108:111], v[124:127]\n"
    "ds_read_b128 v[100:103], v11 offset:17536\n"
    "ds_read_b64_tr_b16 v[76:77], v8 offset:28672\n"
    "s_waitcnt lgkmcnt(9)\n"
    "v_mfma_f32_16x16x32_bf16 v[112:115], v[252:255], v[80:83], v[112:115]\n"
    "ds_read_b128 v[104:107], v11 offset:34048\n"
    "ds_read_b64_tr_b16 v[78:79], v8 offset:29184\n"
    "v_mfma_f32_16x16x32_bf16 v[116:119], v[252:255], v[84:87], v[116:119]\n"
    "ds_read_b128 v[108:111], v11 offset:50560\n"
    "s_waitcnt lgkmcnt(8)\n"
    "v_mfma_f32_16x16x32_bf16 v[120:123], v[252:255], v[88:91], v[120:123]\n"
    "ds_read_b128 v[80:83], v11 offset:1056\n"
    "v_mfma_f32_16x16x32_bf16 v[124:127], v[252:255], v[92:95], v[124:127]\n"
    "ds_read_b128 v[84:87], v11 offset:17568\n"
    "s_waitcnt lgkmcnt(6)\n"
    "v_mfma_f32_16x16x32_bf16 v[112:115], v[224:227], v[96:99], v[112:115]\n"
    "ds_read_b128 v[88:91], v11 offset:34080\n"
    "v_mfma_f32_16x16x32_bf16 v[116:119], v[224:227], v[100:103], v[116:119]\n"
    "ds_read_b128 v[92:95], v11 offset:50592\n"
    "s_waitcnt lgkmcnt(4)\n"
    "v_mfma_f32_16x16x32_bf16 v[120:123], v[224:227], v[104:107], v[120:123]\n"
    "ds_read_b128 v[96:99], v11 offset:1088\n"
    "v_mfma_f32_16x16x32_bf16 v[124:127], v[224:227], v[108:111], v[124:127]\n"
    "ds_read_b128 v[100:103], v11 offset:17600\n"
    "s_waitcnt lgkmcnt(4)\n"
    "v_mfma_f32_16x16x32_bf16 v[112:115], v[228:231], v[80:83], v[112:115]\n"
    "ds_read_b128 v[104:107], v11 offset:34112\n"
    "v_mfma_f32_16x16x32_bf16 v[116:119], v[228:231], v[84:87], v[116:119]\n"
    "ds_read_b128 v[108:111], v11 offset:50624\n"
    "s_waitcnt lgkmcnt(4)\n"
    "v_mfma_f32_16x16x32_bf16 v[120:123], v[228:231], v[88:91], v[120:123]\n"
    "ds_read_b128 v[80:83], v11 offset:1120\n"
    "v_mfma_f32_16x16x32_bf16 v[124:127], v[228:231], v[92:95], v[124:127]\n"
    "ds_read_b128 v[84:87], v11 offset:17632\n"
    "s_waitcnt lgkmcnt(4)\n"
    "v_mfma_f32_16x16x32_bf16 v[112:115], v[232:235], v[96:99], v[112:115]\n"
    "ds_read_b128 v[88:91], v11 offset:34144\n"
    "v_mfma_f32_16x16x32_bf16 v[116:119], v[232:235], v[100:103], v[116:119]\n"
    "ds_read_b128 v[92:95], v11 offset:50656\n"
    "s_waitcnt lgkmcnt(4)\n"
    "v_mfma_f32_16x16x32_bf16 v[120:123], v[232:235], v[104:107], v[120:123]\n"
    "ds_read_b128 v[96:99], v11 offset:1536\n"
    "v_mfma_f32_16x16x32_bf16 v[124:127], v[232:235], v[108:111], v[124:127]\n"
    "ds_read_b128 v[100:103], v11 offset:18048\n"
    "s_waitcnt lgkmcnt(4)\n"
    "v_mfma_f32_16x16x32_bf16 v[112:115], v[236:239], v[80:83], v[112:115]\n"
    "ds_read_b128 v[104:107], v11 offset:34560\n"
    "v_mfma_f32_16x16x32_bf16 v[116:119], v[236:239], v[84:87], v[116:119]\n"
    "ds_read_b128 v[108:111], v11 offset:51072\n"
    "s_waitcnt lgkmcnt(4)\n"
    "v_mfma_f32_16x16x32_bf16 v[120:123], v[236:239], v[88:91], v[120:123]\n"
    "ds_read_b128 v[80:83], v11 offset:1568\n"
    "v_mfma_f32_16x16x32_bf16 v[124:127], v[236:239], v[92:95], v[124:127]\n"
    "ds_read_b128 v[84:87], v11 offset:18080\n"
    "s_waitcnt lgkmcnt(4)\n"
    "v_mfma_f32_16x16x32_bf16 v[112:115], v[64:67], v[96:99], v[112:115]\n"
    "ds_read_b128 v[88:91], v11 offset:34592\n"
    "v_mfma_f32_16x16x32_bf16 v[116:119], v[64:67], v[100:103], v[116:119]\n"
    "ds_read_b128 v[92:95], v11 offset:51104\n"
    "s_waitcnt lgkmcnt(4)\n"
    "v_mfma_f32_16x16x32_bf16 v[120:123], v[64:67], v[104:107], v[120:123]\n"
    "ds_read_b128 v[96:99], v11 offset:1600\n"
    "v_mfma_f32_16x16x32_bf16 v[124:127], v[64:67], v[108:111], v[124:127]\n"
    "ds_read_b128 v[100:103], v11 offset:18112\n"
    "s_waitcnt lgkmcnt(4)\n"
    "v_mfma_f32_16x16x32_bf16 v[112:115], v[68:71], v[80:83], v[112:115]\n"
    "ds_read_b128 v[104:107], v11 offset:34624\n"
    "v_mfma_f32_16x16x32_bf16 v[116:119], v[68:71], v[84:87], v[116:119]\n"
    "ds_read_b128 v[108:111], v11 offset:51136\n"
    "s_waitcnt lgkmcnt(4)\n"
    "v_mfma_f32_16x16x32_bf16 v[120:123], v[68:71], v[88:91], v[120:123]\n"
    "ds_read_b128 v[80:83], v11 offset:1632\n"
    "v_mfma_f32_16x16x32_bf16 v[124:127], v[68:71], v[92:95], v[124:127]\n"
    "ds_read_b128 v[84:87], v11 offset:18144\n"
    "s_waitcnt lgkmcnt(4)\n"
    "v_mfma_f32_16x16x32_bf16 v[112:115], v[72:75], v[96:99], v[112:115]\n"
    "ds_read_b128 v[88:91], v11 offset:34656\n"
    "v_mfma_f32_16x16x32_bf16 v[116:119], v[72:75], v[100:103], v[116:119]\n"
    "ds_read_b128 v[92:95], v11 offset:51168\n"
    "s_waitcnt lgkmcnt(4)\n"
    "v_mfma_f32_16x16x32_bf16 v[120:123], v[72:75], v[104:107], v[120:123]\n"
    "v_mfma_f32_16x16x32_bf16 v[124:127], v[72:75], v[108:111], v[124:127]\n"
    "s_waitcnt lgkmcnt(2)\n"
    "v_mfma_f32_16x16x32_bf16 v[112:115], v[76:79], v[80:83], v[112:115]\n"
    "v_mfma_f32_16x16x32_bf16 v[116:119], v[76:79], v[84:87], v[116:119]\n"
    "s_waitcnt lgkmcnt(0)\n"
    "v_mfma_f32_16x16x32_bf16 v[120:123], v[76:79], v[88:91], v[120:123]\n"
    "v_mfma_f32_16x16x32_bf16 v[124:127], v[76:79], v[92:95], v[124:127]\n"
    "ds_read_b128 v[96:99], v13 offset:5120\n"
    "ds_read_b128 v[100:103], v13 offset:6144\n"
    "ds_read_b128 v[104:107], v13 offset:7168\n"
    "ds_read_b128 v[108:111], v13 offset:8192\n"
    "s_sub_u32 s60, s68, 4096\n"
    "s_cmp_gt_u32 s49, 0\n"
    "s_cselect_b32 s18, s22, 0\n"
    "buffer_load_dwordx4 v[128:131], v13, s[16:19], s60 offen offset:2048 sc1\n"
    "buffer_load_dwordx4 v[160:163], v13, s[16:19], s60 offen offset:3072 sc1\n"
    "v_mov_b64 v[212:213], s[58:59]\n"
    "v_mov_b64 v[214:215], s[58:59]\n"
    "v_mov_b64 v[220:221], s[58:59]\n"
    "v_mov_b64 v[222:223], s[58:59]\n"
    "v_permlane16_swap_b32 v32, v36\n"
    "v_permlane16_swap_b32 v33, v37\n"
    "v_permlane16_swap_b32 v34, v38\n"
    "v_permlane16_swap_b32 v35, v39\n"
    "v_permlane16_swap_b32 v40, v44\n"
    "v_permlane16_swap_b32 v41, v45\n"
    "v_permlane16_swap_b32 v42, v46\n"
    "v_permlane16_swap_b32 v43, v47\n"
    "s_mov_b32 s58, 0x3eb8aa3b\n"
    "buffer_load_dwordx4 v[132:135], v13, s[16:19], s60 offen offset:0 sc1\n"
    "buffer_load_dwordx4 v[168:171], v13, s[16:19], s60 offen offset:1024 sc1\n"
    "buffer_load_dword v56, v4, s[16:19], s9 offen offset:768 sc1\n"
    "v_pk_mul_f32 v[112:113], v[112:113], s[58:59] op_sel_hi:[1,0]\n"
    "v_pk_mul_f32 v[114:115], v[114:115], s[58:59] op_sel_hi:[1,0]\n"
    "v_pk_mul_f32 v[116:117], v[116:117], s[58:59] op_sel_hi:[1,0]\n"
    "v_pk_mul_f32 v[118:119], v[118:119], s[58:59] op_sel_hi:[1,0]\n"
    "v_pk_mul_f32 v[120:121], v[120:121], s[58:59] op_sel_hi:[1,0]\n"
    "v_pk_mul_f32 v[122:123], v[122:123], s[58:59] op_sel_hi:[1,0]\n"
    "v_pk_mul_f32 v[124:125], v[124:125], s[58:59] op_sel_hi:[1,0]\n"
    "v_pk_mul_f32 v[126:127], v[126:127], s[58:59] op_sel_hi:[1,0]\n"
    "v_cvt_pk_bf16_f32 v24, v112, v113\n"
    "v_cvt_pk_bf16_f32 v25, v114, v115\n"
    "v_cvt_pk_bf16_f32 v26, v116, v117\n"
    "v_cvt_pk_bf16_f32 v27, v118, v119\n"
    "v_cvt_pk_bf16_f32 v28, v120, v121\n"
    "v_cvt_pk_bf16_f32 v29, v122, v123\n"
    "v_cvt_pk_bf16_f32 v30, v124, v125\n"
    "v_cvt_pk_bf16_f32 v31, v126, v127\n"
    "v_permlane16_swap_b32 v24, v26\n"
    "v_permlane16_swap_b32 v25, v27\n"
    "v_permlane16_swap_b32 v28, v30\n"
    "v_permlane16_swap_b32_e64 v29, v31\n"
    "s_waitcnt lgkmcnt(0)\n"
    "v_mfma_f32_32x32x16_bf16 v[64:79], v[16:19], v[24:27], v[96:111]\n"
    "s_nop 11\n"
    "v_exp_f32 v64, v64\n"
    "v_exp_f32 v65, v65\n"
    "v_exp_f32 v66, v66\n"
    "v_exp_f32_e64 v67, v67\n"
    "v_cvt_pk_bf16_f32 v112, v64, v65\n"
    "v_exp_f32 v68, v68\n"
    "v_exp_f32 v69, v69\n"
    "v_cvt_pk_bf16_f32 v113, v66, v67\n"
    "v_mfma_f32_32x32x16_bf16 v[80:95], v[20:23], v[28:31], v[96:111]\n"
    "v_exp_f32 v70, v70\n"
    "v_exp_f32 v71, v71\n"
    "v_cvt_pk_bf16_f32 v114, v68, v69\n"
    "v_exp_f32 v72, v72\n"
    "v_exp_f32 v73, v73\n"
    "v_cvt_pk_bf16_f32 v115, v70, v71\n"
    "v_exp_f32 v74, v74\n"
    "v_exp_f32 v75, v75\n"
    "v_mfma_f32_32x32x16_bf16 v[224:239], v[32:35], v[112:115], 0\n"
    "v_cvt_pk_bf16_f32 v116, v72, v73\n"
    "v_exp_f32 v76, v76\n"
    "v_exp_f32 v77, v77\n"
    "v_cvt_pk_bf16_f32 v117, v74, v75\n"
    "v_exp_f32 v78, v78\n"
    "v_exp_f32 v79, v79\n"
    "v_cvt_pk_bf16_f32 v118, v76, v77\n"
    "v_cvt_pk_bf16_f32 v119, v78, v79\n"
    "v_exp_f32 v80, v80\n"
    "v_exp_f32 v81, v81\n"
    "v_mfma_f32_32x32x16_bf16 v[224:239], v[36:39], v[116:119], v[224:239]\n"
    "s_sub_u32 s60, s68, 8192\n"
    "s_cmp_gt_u32 s49, 1\n"
    "s_cselect_b32 s18, s22, 0\n"
    "buffer_load_dwordx4 v[136:139], v13, s[16:19], s60 offen offset:2048 sc1\n"
    "buffer_load_dwordx4 v[176:179], v13, s[16:19], s60 offen offset:3072 sc1\n"
    "v_exp_f32 v82, v82\n"
    "v_exp_f32 v83, v83\n"
    "v_cvt_pk_bf16_f32 v120, v80, v81\n"
    "v_exp_f32 v84, v84\n"
    "v_exp_f32 v85, v85\n"
    "v_cvt_pk_bf16_f32 v121, v82, v83\n"
    "v_mfma_f32_32x32x16_bf16 v[64:79], v[16:19], v[28:31], 0\n"
    "v_exp_f32 v86, v86\n"
    "v_exp_f32 v87, v87\n"
    "v_cvt_pk_bf16_f32 v122, v84, v85\n"
    "v_exp_f32 v88, v88\n"
    "v_exp_f32 v89, v89\n"
    "v_cvt_pk_bf16_f32 v123, v86, v87\n"
    "v_exp_f32 v90, v90\n"
    "v_exp_f32 v91, v91\n"
    "v_mfma_f32_32x32x16_bf16 v[240:255], v[40:43], v[120:123], 0\n"
    "buffer_load_dwordx4 v[140:143], v13, s[16:19], s60 offen offset:0 sc1\n"
    "buffer_load_dwordx4 v[184:187], v13, s[16:19], s60 offen offset:1024 sc1\n"
    "buffer_load_dword v57, v4, s[16:19], s9 offen offset:512 sc1\n"
    "v_cvt_pk_bf16_f32 v124, v88, v89\n"
    "v_exp_f32 v92, v92\n"
    "v_exp_f32 v93, v93\n"
    "v_cvt_pk_bf16_f32 v125, v90, v91\n"
    "v_exp_f32 v94, v94\n"
    "v_exp_f32 v95, v95\n"
    "v_cvt_pk_bf16_f32 v126, v92, v93\n"
    "v_cvt_pk_bf16_f32 v127, v94, v95\n"
    "v_exp_f32 v64, v64\n"
    "v_exp_f32 v65, v65\n"
    "v_mfma_f32_32x32x16_bf16 v[240:255], v[44:47], v[124:127], v[240:255]\n"
    "v_exp_f32 v66, v66\n"
    "v_exp_f32 v67, v67\n"
    "v_cvt_pk_bf16_f32 v112, v64, v65\n"
    "v_exp_f32 v68, v68\n"
    "v_exp_f32 v69, v69\n"
    "v_cvt_pk_bf16_f32 v113, v66, v67\n"
    "v_exp_f32 v70, v70\n"
    "v_exp_f32 v71, v71\n"
    "v_cvt_pk_bf16_f32 v114, v68, v69\n"
    "v_exp_f32 v72, v72\n"
    "v_exp_f32 v73, v73\n"
    "v_cvt_pk_bf16_f32 v115, v70, v71\n"
    "v_exp_f32 v74, v74\n"
    "v_exp_f32 v75, v75\n"
    "v_mfma_f32_32x32x16_bf16 v[240:255], v[32:35], v[112:115], v[240:255]\n"
    "v_cvt_pk_bf16_f32 v116, v72, v73\n"
    "v_exp_f32 v76, v76\n"
    "v_exp_f32 v77, v77\n"
    "v_cvt_pk_bf16_f32 v117, v74, v75\n"
    "v_exp_f32 v78, v78\n"
    "v_exp_f32 v79, v79\n"
    "v_cvt_pk_bf16_f32 v118, v76, v77\n"
    "v_cvt_pk_bf16_f32 v119, v78, v79\n"
    "s_waitcnt vmcnt(9)\n"
    "v_add3_u32 v14, s8, v128, v129\n"
    "v_add3_u32 v14, v14, v130, v131\n"
    "s_waitcnt vmcnt(8)\n"
    "v_add3_u32 v14, v14, v160, v161\n"
    "v_add3_u32 v14, v14, v162, v163\n"
    "s_waitcnt vmcnt(7)\n"
    "v_add3_u32 v14, v14, v132, v133\n"
    "v_add3_u32 v14, v14, v134, v135\n"
    "s_waitcnt vmcnt(6)\n"
    "v_add3_u32 v14, v14, v168, v169\n"
    "v_add3_u32 v14, v14, v170, v171\n"
    "s_waitcnt vmcnt(5)\n"
    "v_cmp_ne_u32 vcc, v14, v56\n"
    "s_mov_b32 s58, 0xf1800000\n"
    "v_sub_f32 v96, s58, v96\n"
    "v_sub_f32 v97, s58, v97\n"
    "v_sub_f32 v98, s58, v98\n"
    "v_sub_f32 v99, s58, v99\n"
    "v_sub_f32 v100, s58, v100\n"
    "v_sub_f32 v101, s58, v101\n"
    "v_sub_f32 v102, s58, v102\n"
    "v_sub_f32_e64 v103, s58, v103\n"
    "s_cbranch_vccnz .Lk_R_kv0\n"
    ".Lk_B_kv0:\n"
    "v_mfma_f32_32x32x16_bf16 v[80:95], v[128:131], v[24:27], 0\n"
    "s_sub_u32 s60, s68, 12288\n"
    "s_cmp_gt_u32 s49, 2\n"
    "s_cselect_b32 s18, s22, 0\n"
    "buffer_load_dwordx4 v[144:147], v13, s[16:19], s60 offen offset:2048 sc1\n"
    "buffer_load_dwordx4 v[192:195], v13, s[16:19], s60 offen offset:3072 sc1\n"
    "v_sub_f32 v104, s58, v104\n"
    "v_sub_f32 v105, s58, v105\n"
    "v_sub_f32 v106, s58, v106\n"
    "v_sub_f32 v107, s58, v107\n"
    "v_sub_f32 v108, s58, v108\n"
    "v_sub_f32 v109, s58, v109\n"
    "v_sub_f32 v110, s58, v110\n"
    "v_sub_f32 v111, s58, v111\n"
    "v_permlane16_swap_b32 v160, v164\n"
    "v_permlane16_swap_b32 v161, v165\n"
    "v_permlane16_swap_b32 v162, v166\n"
    "v_permlane16_swap_b32 v163, v167\n"
    "v_permlane16_swap_b32 v168, v172\n"
    "v_permlane16_swap_b32 v169, v173\n"
    "v_permlane16_swap_b32 v170, v174\n"
    "v_permlane16_swap_b32 v171, v175\n"
    "s_cmp_gt_u32 s49, 0\n"
    "s_cbranch_scc0 .Lk_X_0\n"
    "v_exp_f32 v80, v80\n"
    "v_exp_f32 v81, v81\n"
    "v_mfma_f32_32x32x16_bf16 v[240:255], v[36:39], v[116:119], v[240:255]\n"
    "buffer_load_dwordx4 v[148:151], v13, s[16:19], s60 offen offset:0 sc1\n"
    "buffer_load_dwordx4 v[200:203], v13, s[16:19], s60 offen offset:1024 sc1\n"
    "buffer_load_dword v58, v4, s[16:19], s9 offen offset:256 sc1\n"
    "s_mov_b64 exec, 1\n"
    "ds_add_u32 v15, v15 offset:13259\n"
    "s_mov_b64 exec, -1\n"
    "v_exp_f32 v82, v82\n"
    "v_exp_f32 v83, v83\n"
    "v_cvt_pk_bf16_f32 v120, v80, v81\n"
    "v_exp_f32 v84, v84\n"
    "v_exp_f32 v85, v85\n"
    "v_cvt_pk_bf16_f32 v121, v82, v83\n"
    "v_mfma_f32_32x32x16_bf16 v[64:79], v[128:131], v[28:31], 0\n"
    "v_exp_f32 v86, v86\n"
    "v_exp_f32 v87, v87\n"
    "v_cvt_pk_bf16_f32 v122, v84, v85\n"
    "v_exp_f32 v88, v88\n"
    "v_exp_f32 v89, v89\n"
    "v_cvt_pk_bf16_f32 v123, v86, v87\n"
    "v_exp_f32 v90, v90\n"
    "v_exp_f32 v91, v91\n"
    "v_mfma_f32_32x32x16_bf16 v[224:239], v[160:163], v[120:123], v[224:239]\n"
    "s_sub_u32 s60, s68, 16384\n"
    "s_cmp_gt_u32 s49, 3\n"
    "s_cselect_b32 s18, s22, 0\n"
    "buffer_load_dwordx4 v[152:155], v13, s[16:19], s60 offen offset:2048 sc1\n"
    "buffer_load_dwordx4 v[208:211], v13, s[16:19], s60 offen offset:3072 sc1\n"
    "v_cvt_pk_bf16_f32 v124, v88, v89\n"
    "v_exp_f32 v92, v92\n"
    "v_exp_f32 v93, v93\n"
    "v_cvt_pk_bf16_f32 v125, v90, v91\n"
    "v_exp_f32 v94, v94\n"
    "v_exp_f32 v95, v95\n"
    "v_cvt_pk_bf16_f32 v126, v92, v93\n"
    "v_cvt_pk_bf16_f32 v127, v94, v95\n"
    "v_exp_f32 v64, v64\n"
    "v_exp_f32 v65, v65\n"
    "v_mfma_f32_32x32x16_bf16 v[224:239], v[164:167], v[124:127], v[224:239]\n"
    "v_exp_f32 v66, v66\n"
    "v_exp_f32 v67, v67\n"
    "v_cvt_pk_bf16_f32 v112, v64, v65\n"
    "v_exp_f32 v68, v68\n"
    "v_exp_f32 v69, v69\n"
    "v_cvt_pk_bf16_f32 v113, v66, v67\n"
    "v_mfma_f32_32x32x16_bf16 v[80:95], v[132:135], v[24:27], 0\n"
    "buffer_load_dwordx4 v[156:159], v13, s[16:19], s60 offen offset:0 sc1\n"
    "buffer_load_dwordx4 v[216:219], v13, s[16:19], s60 offen offset:1024 sc1\n"
    "buffer_load_dword v59, v4, s[16:19], s9 offen offset:0 sc1\n"
    "s_mov_b64 exec, 1\n"
    "ds_add_u32 v15, v15 offset:13267\n"
    "s_mov_b64 exec, -1\n"
    "v_exp_f32 v70, v70\n"
    "v_exp_f32 v71, v71\n"
    "v_cvt_pk_bf16_f32 v114, v68, v69\n"
    "v_exp_f32 v72, v72\n"
    "v_exp_f32 v73, v73\n"
    "v_cvt_pk_bf16_f32 v115, v70, v71\n"
    "v_exp_f32 v74, v74\n"
    "v_exp_f32 v75, v75\n"
    "v_mfma_f32_32x32x16_bf16 v[240:255], v[160:163], v[112:115], v[240:255]\n"
    "v_cvt_pk_bf16_f32 v116, v72, v73\n"
    "v_exp_f32 v76, v76\n"
    "v_exp_f32 v77, v77\n"
    "v_cvt_pk_bf16_f32 v117, v74, v75\n"
    "v_exp_f32 v78, v78\n"
    "v_exp_f32 v79, v79\n"
    "v_cvt_pk_bf16_f32 v118, v76, v77\n"
    "v_cvt_pk_bf16_f32 v119, v78, v79\n"
    "v_exp_f32 v80, v80\n"
    "v_exp_f32 v81, v81\n"
    "v_mfma_f32_32x32x16_bf16 v[240:255], v[164:167], v[116:119], v[240:255]\n"
    "v_exp_f32 v82, v82\n"
    "v_exp_f32 v83, v83\n"
    "v_cvt_pk_bf16_f32 v120, v80, v81\n"
    "v_exp_f32 v84, v84\n"
    "v_exp_f32 v85, v85\n"
    "v_cvt_pk_bf16_f32 v121, v82, v83\n"
    "v_mfma_f32_32x32x16_bf16 v[64:79], v[132:135], v[28:31], 0\n"
    "v_exp_f32 v86, v86\n"
    "v_exp_f32 v87, v87\n"
    "v_cvt_pk_bf16_f32 v122, v84, v85\n"
    "v_exp_f32 v88, v88\n"
    "v_exp_f32 v89, v89\n"
    "v_cvt_pk_bf16_f32 v123, v86, v87\n"
    "v_exp_f32 v90, v90\n"
    "v_exp_f32 v91, v91\n"
    "v_mfma_f32_32x32x16_bf16 v[224:239], v[168:171], v[120:123], v[224:239]\n"
    "v_cvt_pk_bf16_f32 v124, v88, v89\n"
    "v_exp_f32 v92, v92\n"
    "v_exp_f32 v93, v93\n"
    "v_cvt_pk_bf16_f32 v125, v90, v91\n"
    "v_exp_f32 v94, v94\n"
    "v_exp_f32 v95, v95\n"
    "v_cvt_pk_bf16_f32 v126, v92, v93\n"
    "v_cvt_pk_bf16_f32 v127, v94, v95\n"
    "v_exp_f32 v64, v64\n"
    "v_exp_f32 v65, v65\n"
    "v_mfma_f32_32x32x16_bf16 v[224:239], v[172:175], v[124:127], v[224:239]\n"
    "v_exp_f32 v66, v66\n"
    "s_waitcnt vmcnt(14)\n"
    "v_add3_u32 v14, s8, v136, v137\n"
    "v_add3_u32 v14, v14, v138, v139\n"
    "s_waitcnt vmcnt(13)\n"
    "v_add3_u32 v14, v14, v176, v177\n"
    "v_add3_u32 v14, v14, v178, v179\n"
    "s_waitcnt vmcnt(12)\n"
    "v_add3_u32 v14, v14, v140, v141\n"
    "v_add3_u32 v14, v14, v142, v143\n"
    "s_waitcnt vmcnt(11)\n"
    "v_add3_u32 v14, v14, v184, v185\n"
    "v_add3_u32 v14, v14, v186, v187\n"
    "s_waitcnt vmcnt(10)\n"
    "v_cmp_ne_u32 vcc, v14, v57\n"
    "v_exp_f32 v67, v67\n"
    "v_cvt_pk_bf16_f32 v112, v64, v65\n"
    "v_exp_f32 v68, v68\n"
    "v_exp_f32 v69, v69\n"
    "v_cvt_pk_bf16_f32 v113, v66, v67\n"
    "s_cbranch_vccnz .Lk_R_kv1\n"
    ".Lk_B_kv1:\n"
    "v_mfma_f32_32x32x16_bf16 v[80:95], v[136:139], v[24:27], 0\n"
    "v_permlane16_swap_b32 v176, v180\n"
    "v_permlane16_swap_b32 v177, v181\n"
    "v_permlane16_swap_b32 v178, v182\n"
    "v_permlane16_swap_b32 v179, v183\n"
    "v_permlane16_swap_b32 v184, v188\n"
    "v_permlane16_swap_b32 v185, v189\n"
    "v_permlane16_swap_b32 v186, v190\n"
    "v_permlane16_swap_b32 v187, v191\n"
    "v_exp_f32 v70, v70\n"
    "v_exp_f32_e64 v71, v71\n"
    "v_cvt_pk_bf16_f32 v114, v68, v69\n"
    "v_exp_f32 v72, v72\n"
    "v_exp_f32 v73, v73\n"
    "v_cvt_pk_bf16_f32 v115, v70, v71\n"
    "v_exp_f32 v74, v74\n"
    "v_exp_f32 v75, v75\n"
    "v_mfma_f32_32x32x16_bf16 v[240:255], v[168:171], v[112:115], v[240:255]\n"
    "v_cvt_pk_bf16_f32 v116, v72, v73\n"
    "v_exp_f32 v76, v76\n"
    "v_exp_f32 v77, v77\n"
    "v_cvt_pk_bf16_f32 v117, v74, v75\n"
    "v_exp_f32 v78, v78\n"
    "v_exp_f32 v79, v79\n"
    "v_cvt_pk_bf16_f32 v118, v76, v77\n"
    "v_cvt_pk_bf16_f32 v119, v78, v79\n"
    "s_cmp_gt_u32 s49, 1\n"
    "s_cbranch_scc0 .Lk_X_1\n"
    "v_exp_f32 v80, v80\n"
    "v_exp_f32 v81, v81\n"
    "v_mfma_f32_32x32x16_bf16 v[240:255], v[172:175], v[116:119], v[240:255]\n"
    "v_exp_f32 v82, v82\n"
    "v_exp_f32 v83, v83\n"
    "v_cvt_pk_bf16_f32 v120, v80, v81\n"
    "v_exp_f32 v84, v84\n"
    "v_exp_f32 v85, v85\n"
    "v_cvt_pk_bf16_f32 v121, v82, v83\n"
    "v_mfma_f32_32x32x16_bf16 v[64:79], v[136:139], v[28:31], 0\n"
    "v_exp_f32 v86, v86\n"
    "v_exp_f32 v87, v87\n"
    "v_cvt_pk_bf16_f32 v122, v84, v85\n"
    "v_exp_f32 v88, v88\n"
    "v_exp_f32 v89, v89\n"
    "v_cvt_pk_bf16_f32 v123, v86, v87\n"
    "v_exp_f32 v90, v90\n"
    "v_exp_f32 v91, v91\n"
    "v_mfma_f32_32x32x16_bf16 v[224:239], v[176:179], v[120:123], v[224:239]\n"
    "v_cvt_pk_bf16_f32 v124, v88, v89\n"
    "v_exp_f32 v92, v92\n"
    "v_exp_f32 v93, v93\n"
    "v_cvt_pk_bf16_f32 v125, v90, v91\n"
    "v_exp_f32 v94, v94\n"
    "v_exp_f32 v95, v95\n"
    "v_cvt_pk_bf16_f32 v126, v92, v93\n"
    "v_cvt_pk_bf16_f32 v127, v94, v95\n"
    "v_exp_f32 v64, v64\n"
    "v_exp_f32 v65, v65\n"
    "v_mfma_f32_32x32x16_bf16 v[224:239], v[180:183], v[124:127], v[224:239]\n"
    "v_exp_f32 v66, v66\n"
    "v_exp_f32 v67, v67\n"
    "v_cvt_pk_bf16_f32 v112, v64, v65\n"
    "v_exp_f32 v68, v68\n"
    "v_exp_f32 v69, v69\n"
    "v_cvt_pk_bf16_f32 v113, v66, v67\n"
    "v_mfma_f32_32x32x16_bf16 v[80:95], v[140:143], v[24:27], 0\n"
    "v_exp_f32 v70, v70\n"
    "v_exp_f32 v71, v71\n"
    "v_cvt_pk_bf16_f32 v114, v68, v69\n"
    "v_exp_f32 v72, v72\n"
    "v_exp_f32 v73, v73\n"
    "v_cvt_pk_bf16_f32 v115, v70, v71\n"
    "v_exp_f32 v74, v74\n"
    "v_exp_f32 v75, v75\n"
    "v_mfma_f32_32x32x16_bf16 v[240:255], v[176:179], v[112:115], v[240:255]\n"
    "v_cvt_pk_bf16_f32 v116, v72, v73\n"
    "v_exp_f32 v76, v76\n"
    "v_exp_f32 v77, v77\n"
    "v_cvt_pk_bf16_f32 v117, v74, v75\n"
    "v_exp_f32 v78, v78\n"
    "v_exp_f32 v79, v79\n"
    "v_cvt_pk_bf16_f32 v118, v76, v77\n"
    "v_cvt_pk_bf16_f32 v119, v78, v79\n"
    "v_exp_f32 v80, v80\n"
    "v_exp_f32 v81, v81\n"
    "v_mfma_f32_32x32x16_bf16 v[240:255], v[180:183], v[116:119], v[240:255]\n"
    "v_exp_f32 v82, v82\n"
    "v_exp_f32 v83, v83\n"
    "v_cvt_pk_bf16_f32 v120, v80, v81\n"
    "v_exp_f32 v84, v84\n"
    "v_exp_f32 v85, v85\n"
    "v_cvt_pk_bf16_f32 v121, v82, v83\n"
    "v_mfma_f32_32x32x16_bf16 v[64:79], v[140:143], v[28:31], 0\n"
    "v_exp_f32 v86, v86\n"
    "v_exp_f32 v87, v87\n"
    "v_cvt_pk_bf16_f32 v122, v84, v85\n"
    "v_exp_f32 v88, v88\n"
    "v_exp_f32 v89, v89\n"
    "v_cvt_pk_bf16_f32 v123, v86, v87\n"
    "v_exp_f32 v90, v90\n"
    "v_exp_f32 v91, v91\n"
    "v_mfma_f32_32x32x16_bf16 v[224:239], v[184:187], v[120:123], v[224:239]\n"
    "v_cvt_pk_bf16_f32 v124, v88, v89\n"
    "v_exp_f32 v92, v92\n"
    "v_exp_f32 v93, v93\n"
    "v_cvt_pk_bf16_f32 v125, v90, v91\n"
    "v_exp_f32 v94, v94\n"
    "v_exp_f32 v95, v95\n"
    "v_cvt_pk_bf16_f32 v126, v92, v93\n"
    "v_cvt_pk_bf16_f32 v127, v94, v95\n"
    "v_exp_f32 v64, v64\n"
    "v_exp_f32 v65, v65\n"
    "v_mfma_f32_32x32x16_bf16 v[224:239], v[188:191], v[124:127], v[224:239]\n"
    "v_exp_f32 v66, v66\n"
    "s_waitcnt vmcnt(9)\n"
    "v_add3_u32 v14, s8, v144, v145\n"
    "v_add3_u32 v14, v14, v146, v147\n"
    "s_waitcnt vmcnt(8)\n"
    "v_add3_u32 v14, v14, v192, v193\n"
    "v_add3_u32 v14, v14, v194, v195\n"
    "s_waitcnt vmcnt(7)\n"
    "v_add3_u32 v14, v14, v148, v149\n"
    "v_add3_u32 v14, v14, v150, v151\n"
    "s_waitcnt vmcnt(6)\n"
    "v_add3_u32 v14, v14, v200, v201\n"
    "v_add3_u32 v14, v14, v202, v203\n"
    "s_waitcnt vmcnt(5)\n"
    "v_cmp_ne_u32 vcc, v14, v58\n"
    "v_exp_f32 v67, v67\n"
    "v_cvt_pk_bf16_f32 v112, v64, v65\n"
    "v_exp_f32 v68, v68\n"
    "v_exp_f32 v69, v69\n"
    "v_cvt_pk_bf16_f32 v113, v66, v67\n"
    "s_cbranch_vccnz .Lk_R_kv2\n"
    ".Lk_B_kv2:\n"
    "v_mfma_f32_32x32x16_bf16 v[80:95], v[144:147], v[24:27], 0\n"
    "v_permlane16_swap_b32 v192, v196\n"
    "v_permlane16_swap_b32 v193, v197\n"
    "v_permlane16_swap_b32 v194, v198\n"
    "v_permlane16_swap_b32 v195, v199\n"
    "v_permlane16_swap_b32 v200, v204\n"
    "v_permlane16_swap_b32 v201, v205\n"
    "v_permlane16_swap_b32 v202, v206\n"
    "v_permlane16_swap_b32 v203, v207\n"
    "v_exp_f32 v70, v70\n"
    "v_exp_f32_e64 v71, v71\n"
    "v_cvt_pk_bf16_f32 v114, v68, v69\n"
    "v_exp_f32 v72, v72\n"
    "v_exp_f32 v73, v73\n"
    "v_cvt_pk_bf16_f32 v115, v70, v71\n"
    "v_exp_f32 v74, v74\n"
    "v_exp_f32 v75, v75\n"
    "v_mfma_f32_32x32x16_bf16 v[240:255], v[184:187], v[112:115], v[240:255]\n"
    "v_cvt_pk_bf16_f32 v116, v72, v73\n"
    "v_exp_f32 v76, v76\n"
    "v_exp_f32 v77, v77\n"
    "v_cvt_pk_bf16_f32 v117, v74, v75\n"
    "v_exp_f32 v78, v78\n"
    "v_exp_f32 v79, v79\n"
    "v_cvt_pk_bf16_f32 v118, v76, v77\n"
    "v_cvt_pk_bf16_f32 v119, v78, v79\n"
    "s_cmp_gt_u32 s49, 2\n"
    "s_cbranch_scc0 .Lk_X_2\n"
    "v_exp_f32 v80, v80\n"
    "v_exp_f32 v81, v81\n"
    "v_mfma_f32_32x32x16_bf16 v[240:255], v[188:191], v[116:119], v[240:255]\n"
    "v_exp_f32 v82, v82\n"
    "v_exp_f32 v83, v83\n"
    "v_cvt_pk_bf16_f32 v120, v80, v81\n"
    "v_exp_f32 v84, v84\n"
    "v_exp_f32 v85, v85\n"
    "v_cvt_pk_bf16_f32 v121, v82, v83\n"
    "v_mfma_f32_32x32x16_bf16 v[64:79], v[144:147], v[28:31], 0\n"
    "v_exp_f32 v86, v86\n"
    "v_exp_f32 v87, v87\n"
    "v_cvt_pk_bf16_f32 v122, v84, v85\n"
    "v_exp_f32 v88, v88\n"
    "v_exp_f32 v89, v89\n"
    "v_cvt_pk_bf16_f32 v123, v86, v87\n"
    "v_exp_f32 v90, v90\n"
    "v_exp_f32 v91, v91\n"
    "v_mfma_f32_32x32x16_bf16 v[224:239], v[192:195], v[120:123], v[224:239]\n"
    "v_cvt_pk_bf16_f32 v124, v88, v89\n"
    "v_exp_f32 v92, v92\n"
    "v_exp_f32 v93, v93\n"
    "v_cvt_pk_bf16_f32 v125, v90, v91\n"
    "v_exp_f32 v94, v94\n"
    "v_exp_f32 v95, v95\n"
    "v_cvt_pk_bf16_f32 v126, v92, v93\n"
    "v_cvt_pk_bf16_f32 v127, v94, v95\n"
    "v_exp_f32 v64, v64\n"
    "v_exp_f32 v65, v65\n"
    "v_mfma_f32_32x32x16_bf16 v[224:239], v[196:199], v[124:127], v[224:239]\n"
    "v_exp_f32 v66, v66\n"
    "v_exp_f32 v67, v67\n"
    "v_cvt_pk_bf16_f32 v112, v64, v65\n"
    "v_exp_f32 v68, v68\n"
    "v_exp_f32 v69, v69\n"
    "v_cvt_pk_bf16_f32 v113, v66, v67\n"
    "v_mfma_f32_32x32x16_bf16 v[80:95], v[148:151], v[24:27], 0\n"
    "v_exp_f32 v70, v70\n"
    "v_exp_f32 v71, v71\n"
    "v_cvt_pk_bf16_f32 v114, v68, v69\n"
    "v_exp_f32 v72, v72\n"
    "v_exp_f32 v73, v73\n"
    "v_cvt_pk_bf16_f32 v115, v70, v71\n"
    "v_exp_f32 v74, v74\n"
    "v_exp_f32 v75, v75\n"
    "v_mfma_f32_32x32x16_bf16 v[240:255], v[192:195], v[112:115], v[240:255]\n"
    "v_cvt_pk_bf16_f32 v116, v72, v73\n"
    "v_exp_f32 v76, v76\n"
    "v_exp_f32 v77, v77\n"
    "v_cvt_pk_bf16_f32 v117, v74, v75\n"
    "v_exp_f32 v78, v78\n"
    "v_exp_f32 v79, v79\n"
    "v_cvt_pk_bf16_f32 v118, v76, v77\n"
    "v_cvt_pk_bf16_f32 v119, v78, v79\n"
    "v_exp_f32 v80, v80\n"
    "v_exp_f32 v81, v81\n"
    "v_mfma_f32_32x32x16_bf16 v[240:255], v[196:199], v[116:119], v[240:255]\n"
    "v_exp_f32 v82, v82\n"
    "v_exp_f32 v83, v83\n"
    "v_cvt_pk_bf16_f32 v120, v80, v81\n"
    "v_exp_f32 v84, v84\n"
    "v_exp_f32 v85, v85\n"
    "v_cvt_pk_bf16_f32 v121, v82, v83\n"
    "v_mfma_f32_32x32x16_bf16 v[64:79], v[148:151], v[28:31], 0\n"
    "v_exp_f32 v86, v86\n"
    "v_exp_f32 v87, v87\n"
    "v_cvt_pk_bf16_f32 v122, v84, v85\n"
    "v_exp_f32 v88, v88\n"
    "v_exp_f32 v89, v89\n"
    "v_cvt_pk_bf16_f32 v123, v86, v87\n"
    "v_exp_f32 v90, v90\n"
    "v_exp_f32 v91, v91\n"
    "v_mfma_f32_32x32x16_bf16 v[224:239], v[200:203], v[120:123], v[224:239]\n"
    "v_cvt_pk_bf16_f32 v124, v88, v89\n"
    "v_exp_f32 v92, v92\n"
    "v_exp_f32 v93, v93\n"
    "v_cvt_pk_bf16_f32 v125, v90, v91\n"
    "v_exp_f32 v94, v94\n"
    "v_exp_f32 v95, v95\n"
    "v_cvt_pk_bf16_f32 v126, v92, v93\n"
    "v_cvt_pk_bf16_f32 v127, v94, v95\n"
    "v_exp_f32 v64, v64\n"
    "v_exp_f32 v65, v65\n"
    "v_mfma_f32_32x32x16_bf16 v[224:239], v[204:207], v[124:127], v[224:239]\n"
    "v_exp_f32 v66, v66\n"
    "s_waitcnt vmcnt(4)\n"
    "v_add3_u32 v14, s8, v152, v153\n"
    "v_add3_u32 v14, v14, v154, v155\n"
    "s_waitcnt vmcnt(3)\n"
    "v_add3_u32 v14, v14, v208, v209\n"
    "v_add3_u32 v14, v14, v210, v211\n"
    "s_waitcnt vmcnt(2)\n"
    "v_add3_u32 v14, v14, v156, v157\n"
    "v_add3_u32 v14, v14, v158, v159\n"
    "s_waitcnt vmcnt(1)\n"
    "v_add3_u32 v14, v14, v216, v217\n"
    "v_add3_u32 v14, v14, v218, v219\n"
    "s_waitcnt vmcnt(0)\n"
    "v_cmp_ne_u32 vcc, v14, v59\n"
    "v_exp_f32 v67, v67\n"
    "v_cvt_pk_bf16_f32 v112, v64, v65\n"
    "v_exp_f32 v68, v68\n"
    "v_exp_f32 v69, v69\n"
    "v_cvt_pk_bf16_f32 v113, v66, v67\n"
    "s_cbranch_vccnz .Lk_R_kv3\n"
    ".Lk_B_kv3:\n"
    "v_mfma_f32_32x32x16_bf16 v[80:95], v[152:155], v[28:31], v[96:111]\n"
    "v_permlane16_swap_b32 v208, v212\n"
    "v_permlane16_swap_b32 v209, v213\n"
    "v_permlane16_swap_b32 v210, v214\n"
    "v_permlane16_swap_b32 v211, v215\n"
    "v_permlane16_swap_b32 v216, v220\n"
    "v_permlane16_swap_b32 v217, v221\n"
    "v_permlane16_swap_b32 v218, v222\n"
    "v_permlane16_swap_b32 v219, v223\n"
    "v_exp_f32 v70, v70\n"
    "v_exp_f32_e64 v71, v71\n"
    "v_cvt_pk_bf16_f32 v114, v68, v69\n"
    "v_exp_f32 v72, v72\n"
    "v_exp_f32 v73, v73\n"
    "v_cvt_pk_bf16_f32 v115, v70, v71\n"
    "v_exp_f32 v74, v74\n"
    "v_exp_f32 v75, v75\n"
    "v_mfma_f32_32x32x16_bf16 v[240:255], v[200:203], v[112:115], v[240:255]\n"
    "v_cvt_pk_bf16_f32 v116, v72, v73\n"
    "v_exp_f32 v76, v76\n"
    "v_exp_f32 v77, v77\n"
    "v_cvt_pk_bf16_f32 v117, v74, v75\n"
    "v_exp_f32 v78, v78\n"
    "v_exp_f32 v79, v79\n"
    "v_cvt_pk_bf16_f32 v118, v76, v77\n"
    "v_cvt_pk_bf16_f32 v119, v78, v79\n"
    "s_cmp_gt_u32 s49, 3\n"
    "s_cbranch_scc0 .Lk_X_3\n"
    "v_exp_f32 v80, v80\n"
    "v_exp_f32 v81, v81\n"
    "v_mfma_f32_32x32x16_bf16 v[240:255], v[204:207], v[116:119], v[240:255]\n"
    "v_exp_f32 v82, v82\n"
    "v_exp_f32 v83, v83\n"
    "v_cvt_pk_bf16_f32 v120, v80, v81\n"
    "v_exp_f32 v84, v84\n"
    "v_exp_f32 v85, v85\n"
    "v_cvt_pk_bf16_f32 v121, v82, v83\n"
    "v_mfma_f32_32x32x16_bf16 v[64:79], v[152:155], v[24:27], 0\n"
    "v_exp_f32 v86, v86\n"
    "v_exp_f32 v87, v87\n"
    "v_cvt_pk_bf16_f32 v122, v84, v85\n"
    "v_exp_f32 v88, v88\n"
    "v_exp_f32 v89, v89\n"
    "v_cvt_pk_bf16_f32 v123, v86, v87\n"
    "v_exp_f32 v90, v90\n"
    "v_exp_f32 v91, v91\n"
    "v_mfma_f32_32x32x16_bf16 v[240:255], v[208:211], v[120:123], v[240:255]\n"
    "v_cvt_pk_bf16_f32 v124, v88, v89\n"
    "v_exp_f32 v92, v92\n"
    "v_exp_f32 v93, v93\n"
    "v_cvt_pk_bf16_f32 v125, v90, v91\n"
    "v_exp_f32 v94, v94\n"
    "v_exp_f32 v95, v95\n"
    "v_cvt_pk_bf16_f32 v126, v92, v93\n"
    "v_cvt_pk_bf16_f32 v127, v94, v95\n"
    "v_exp_f32 v64, v64\n"
    "v_exp_f32 v65, v65\n"
    "v_mfma_f32_32x32x16_bf16 v[240:255], v[212:215], v[124:127], v[240:255]\n"
    "v_exp_f32 v66, v66\n"
    "v_exp_f32 v67, v67\n"
    "v_cvt_pk_bf16_f32 v112, v64, v65\n"
    "v_exp_f32 v68, v68\n"
    "v_exp_f32 v69, v69\n"
    "v_cvt_pk_bf16_f32 v113, v66, v67\n"
    "v_mfma_f32_32x32x16_bf16 v[80:95], v[156:159], v[24:27], v[96:111]\n"
    "v_exp_f32 v70, v70\n"
    "v_exp_f32 v71, v71\n"
    "v_cvt_pk_bf16_f32 v114, v68, v69\n"
    "v_exp_f32 v72, v72\n"
    "v_exp_f32 v73, v73\n"
    "v_cvt_pk_bf16_f32 v115, v70, v71\n"
    "v_exp_f32 v74, v74\n"
    "v_exp_f32 v75, v75\n"
    "v_mfma_f32_32x32x16_bf16 v[224:239], v[208:211], v[112:115], v[224:239]\n"
    "v_cvt_pk_bf16_f32 v116, v72, v73\n"
    "v_exp_f32 v76, v76\n"
    "v_exp_f32 v77, v77\n"
    "v_cvt_pk_bf16_f32 v117, v74, v75\n"
    "v_exp_f32 v78, v78\n"
    "v_exp_f32 v79, v79\n"
    "v_cvt_pk_bf16_f32 v118, v76, v77\n"
    "v_cvt_pk_bf16_f32 v119, v78, v79\n"
    "v_exp_f32 v80, v80\n"
    "v_exp_f32 v81, v81\n"
    "v_mfma_f32_32x32x16_bf16 v[224:239], v[212:215], v[116:119], v[224:239]\n"
    "v_exp_f32 v82, v82\n"
    "v_exp_f32 v83, v83\n"
    "v_cvt_pk_bf16_f32 v120, v80, v81\n"
    "v_exp_f32 v84, v84\n"
    "v_exp_f32 v85, v85\n"
    "v_cvt_pk_bf16_f32 v121, v82, v83\n"
    "v_exp_f32 v86, v86\n"
    "v_exp_f32 v87, v87\n"
    "v_cvt_pk_bf16_f32 v122, v84, v85\n"
    "v_exp_f32 v88, v88\n"
    "v_exp_f32 v89, v89\n"
    "v_cvt_pk_bf16_f32 v123, v86, v87\n"
    "v_exp_f32 v90, v90\n"
    "v_exp_f32 v91, v91\n"
    "v_mfma_f32_32x32x16_bf16 v[224:239], v[216:219], v[120:123], v[224:239]\n"
    "v_cvt_pk_bf16_f32 v124, v88, v89\n"
    "v_exp_f32 v92, v92\n"
    "v_exp_f32 v93, v93\n"
    "v_cvt_pk_bf16_f32 v125, v90, v91\n"
    "v_exp_f32 v94, v94\n"
    "v_exp_f32 v95, v95\n"
    "v_cvt_pk_bf16_f32 v126, v92, v93\n"
    "v_cvt_pk_bf16_f32 v127, v94, v95\n"
    "s_nop 1\n"
    "v_mfma_f32_32x32x16_bf16 v[224:239], v[220:223], v[124:127], v[224:239]\n"
    ".Lk_Latt_end:\n"
    "s_setprio 3\n"
    "s_mov_b32 s64, 0\n"
    ".Lk_Latt_norm:\n"
    "s_mov_b32 s58, 0x5d800000\n"
    "s_mov_b32 s59, 0x21800000\n"
    "s_add_u32 s65, s69, 32768\n"
    "s_lshr_b32 s66, s69, 3\n"
    "s_add_u32 s66, s66, 3538944\n"
    "s_add_u32 s67, s66, 4096\n"
    "v_rcp_f32 v66, v248\n"
    "s_nop 0\n"
    "v_mul_f32 v240, v240, v66\n"
    "v_mul_f32 v241, v241, v66\n"
    "v_mul_f32 v242, v242, v66\n"
    "v_mul_f32 v243, v243, v66\n"
    "v_mul_f32 v244, v244, v66\n"
    "v_mul_f32 v245, v245, v66\n"
    "v_mul_f32 v246, v246, v66\n"
    "v_mul_f32 v247, v247, v66\n"
    "v_cvt_pk_bf16_f32 v72, v240, v241\n"
    "v_cvt_pk_bf16_f32 v73, v242, v243\n"
    "v_cvt_pk_bf16_f32 v74, v244, v245\n"
    "v_cvt_pk_bf16_f32 v75, v246, v247\n"
    "v_add3_u32 v67, s8, v72, v73\n"
    "v_add3_u32 v67, v67, v74, v75\n"
    "v_rcp_f32_e64 v64, v232\n"
    "v_minimum3_f32 v76, v232, v248, v248\n"
    "v_max_f32 v77, v232, v248\n"
    "v_cmp_nlt_f32 vcc, s59, v76\n"
    "v_cmp_ngt_f32 s[60:61], s58, v77\n"
    "v_mul_f32 v224, v224, v64\n"
    "v_mul_f32 v225, v225, v64\n"
    "v_mul_f32 v226, v226, v64\n"
    "v_mul_f32 v227, v227, v64\n"
    "v_mul_f32 v228, v228, v64\n"
    "v_mul_f32 v229, v229, v64\n"
    "v_mul_f32 v230, v230, v64\n"
    "v_mul_f32 v231, v231, v64\n"
    "v_cvt_pk_bf16_f32 v68, v224, v225\n"
    "v_cvt_pk_bf16_f32 v69, v226, v227\n"
    "v_cvt_pk_bf16_f32 v70, v228, v229\n"
    "v_cvt_pk_bf16_f32 v71, v230, v231\n"
    "v_add3_u32 v65, s8, v68, v69\n"
    "v_add3_u32 v65, v65, v70, v71\n"
    "s_or_b64 vcc, vcc, s[60:61]\n"
    "s_cbranch_vccnz .Lk_Lfallback\n"
    ".Lk_Latt_pub:\n"
    "buffer_store_dwordx4 v[72:75], v13, s[20:23], s65 offen sc1\n"
    "v_add_u32_dpp v67, v67, v67 quad_perm:[1,0,3,2] row_mask:0xf bank_mask:0xf\n"
    "v_add_u32_dpp v65, v65, v65 quad_perm:[1,0,3,2] row_mask:0xf bank_mask:0xf\n"
    "buffer_store_dwordx4 v[68:71], v13, s[20:23], s69 offen sc1\n"
    "v_add_u32_dpp v67, v67, v67 quad_perm:[2,3,0,1] row_mask:0xf bank_mask:0xf\n"
    "v_add_u32_dpp v65, v65, v65 quad_perm:[2,3,0,1] row_mask:0xf bank_mask:0xf\n"
    "s_mov_b32 exec_lo, 0x11111111\n"
    "s_mov_b32 exec_hi, 0x11111111\n"
    "buffer_store_dword v67, v10, s[20:23], s67 offen sc1\n"
    "buffer_store_dword v65, v10, s[20:23], s66 offen sc1\n"
    "s_mov_b64 exec, -1\n"
    "s_lshr_b32 s51, s57, 2\n"
    "s_lshl1_add_u32 s58, s48, s51\n"
    "s_and_b32 s59, s58, 7\n"
    "s_add_u32 s58, s58, 1\n"
    "s_and_b32 s58, s58, 7\n"
    "s_lshl3_add_u32 s59, s52, s59\n"
    "s_lshl3_add_u32 s58, s52, s58\n"
    "s_lshl_b32 s59, s59, 12\n"
    "s_lshl_b32 s58, s58, 12\n"
    "s_add_u32 s66, s59, 1048576\n"
    "s_add_u32 s67, s58, 1048576\n"
    "s_lshr_b32 s59, s59, 3\n"
    "s_lshr_b32 s58, s58, 3\n"
    "v_and_b32_e64 v14, 3, v1\n"
    "v_lshl_add_u32 v61, v14, 7, v10\n"
    "s_add_u32 s64, s59, 3670016\n"
    "s_add_u32 s65, s58, 3670016\n"
    "s_lshl_b32 s10, s8, 2\n"
    "s_lshl_b32 s58, s48, 13\n"
    "v_add_u32 v15, s58, v13\n"
    "s_lshr_b32 s58, s57, 3\n"
    "s_lshl3_add_u32 s58, s58, s56\n"
    "s_and_b32 s59, s57, 7\n"
    "s_cselect_b32 s58, s58, 99\n"
    "s_sub_u32 s17, s59, 1\n"
    "s_cmp_eq_u32 s48, 3\n"
    "s_cselect_b32 s17, s17, s48\n"
    "s_cselect_b32 s59, 0, s59\n"
    "v_lshl_add_u32 v14, v1, 3, s59\n"
    "s_cmp_eq_u32 s17, 0\n"
    "s_cselect_b64 s[4:5], s[36:37], s[40:41]\n"
    "s_cmp_eq_u32 s17, 2\n"
    "s_cselect_b64 s[4:5], s[44:45], s[4:5]\n"
    "s_cselect_b32 s59, 16, 7\n"
    "s_cselect_b32 s60, 1, 28\n"
    "s_lshl_b32 s59, s58, s59\n"
    "s_cmp_lt_u32 s58, 28\n"
    "s_cselect_b32 s6, s38, 0\n"
    "s_cmp_gt_u32 s17, 2\n"
    "s_cselect_b32 s6, 0, s6\n"
    "v_mul_u32_u24 v14, s60, v14\n"
    "v_lshl_add_u32 v62, v14, 7, s59\n"
    "s_barrier\n"
    "s_cmp_lt_i32 s54, 0\n"
    "s_cbranch_scc1 .Lk_Lnoprep\n"
    "v_lshrrev_b32 v26, 3, v0\n"
    "s_lshl_b32 s58, s48, 3\n"
    "v_add_u32 v26, s58, v26\n"
    "v_mul_u32_u24 v26, 3584, v26\n"
    "v_bfe_u32 v16, v0, 4, 1\n"
    "v_lshlrev_b32 v16, 1, v16\n"
    "v_and_b32 v17, 7, v0\n"
    "v_xor_b32 v16, v16, v17\n"
    "s_lshl_b32 s58, s54, 7\n"
    "v_lshl_add_u32 v16, v16, 4, s58\n"
    "v_add_u32 v16, v16, v26\n"
    "v_xor_b32 v17, 64, v16\n"
    "v_add_u32 v17, 28672, v17\n"
    "s_add_u32 m0, s98, 114688\n"
    "s_mov_b32 s3, 0\n"
    "buffer_load_dwordx4 v16, s[36:39], s3 offen lds\n"
    "s_add_u32 m0, s98, 115712\n"
    "s_mov_b32 s3, 0\n"
    "buffer_load_dwordx4 v16, s[40:43], s3 offen lds\n"
    "s_add_u32 m0, s98, 116736\n"
    "s_mov_b32 s3, 0\n"
    "buffer_load_dwordx4 v17, s[36:39], s3 offen lds\n"
    "s_add_u32 m0, s98, 117760\n"
    "s_mov_b32 s3, 0\n"
    "buffer_load_dwordx4 v17, s[40:43], s3 offen lds\n"
    "s_add_u32 m0, s98, 131072\n"
    "s_mov_b32 s3, 229376\n"
    "buffer_load_dwordx4 v16, s[36:39], s3 offen lds\n"
    "s_add_u32 m0, s98, 132096\n"
    "s_mov_b32 s3, 229376\n"
    "buffer_load_dwordx4 v16, s[40:43], s3 offen lds\n"
    "s_add_u32 m0, s98, 133120\n"
    "s_mov_b32 s3, 229376\n"
    "buffer_load_dwordx4 v17, s[36:39], s3 offen lds\n"
    "s_add_u32 m0, s98, 134144\n"
    "s_mov_b32 s3, 229376\n"
    "buffer_load_dwordx4 v17, s[40:43], s3 offen lds\n"
    "s_add_u32 m0, s98, 65024\n"
    "s_mov_b32 s3, 458752\n"
    "buffer_load_dwordx4 v16, s[36:39], s3 offen lds\n"
    "s_add_u32 m0, s98, 66048\n"
    "s_mov_b32 s3, 458752\n"
    "buffer_load_dwordx4 v16, s[40:43], s3 offen lds\n"
    "s_add_u32 m0, s98, 67072\n"
    "s_mov_b32 s3, 458752\n"
    "buffer_load_dwordx4 v17, s[36:39], s3 offen lds\n"
    "s_add_u32 m0, s98, 68096\n"
    "s_mov_b32 s3, 458752\n"
    "buffer_load_dwordx4 v17, s[40:43], s3 offen lds\n"
    "v_xor_b32 v18, 0, v13\n"
    "v_xor_b32 v19, 16, v13\n"
    "v_xor_b32 v20, 32, v13\n"
    "v_xor_b32 v21, 48, v13\n"
    "s_lshl3_add_u32 s99, s54, s48\n"
    "s_lshl_b32 s99, s99, 13\n"
    "s_lshl_b32 s0, s48, 13\n"
    "v_lshrrev_b32 v22, 4, v1\n"
    "v_bfe_u32 v23, v1, 2, 2\n"
    "v_and_b32 v24, 3, v1\n"
    "v_lshlrev_b32 v24, 3, v24\n"
    "v_and_b32 v25, 1, v22\n"
    "v_lshrrev_b32 v5, 1, v23\n"
    "v_lshl_or_b32 v5, v25, 1, v5\n"
    "v_xor_b32_e64 v5, s48, v5\n"
    "v_lshl_add_u32 v5, v5, 5, v24\n"
    "v_lshl_add_u32 v5, v23, 7, v5\n"
    "v_lshl_add_u32 v5, v22, 11, v5\n"
    "v_add_u32 v7, 146944, v5\n"
    "v_add_u32 v6, 81408, v5\n"
    "v_add_u32 v5, 15872, v5\n"
    "v_lshlrev_b32_e64 v25, 3, v24\n"
    "v_lshl_add_u32 v25, v23, 4, v25\n"
    "v_lshl_add_u32 v25, v23, 10, v25\n"
    "v_lshl_add_u32 v25, v22, 13, v25\n"
    "s_lshl_b32 s58, s48, 8\n"
    "s_add_u32 s58, s58, 15872\n"
    "v_add_u32 v40, s58, v25\n"
    "v_xor_b32 v41, 8, v40\n"
    "v_xor_b32 v42, 16, v40\n"
    "v_xor_b32 v43, 24, v40\n"
    "v_xor_b32 v44, 32, v40\n"
    "v_xor_b32 v45, 40, v40\n"
    "v_xor_b32 v46, 48, v40\n"
    "v_xor_b32 v47, 56, v40\n"
    "v_mov_b32 v26, 0\n"
    "v_lshlrev_b32_e64 v27, 2, v2\n"
    "ds_write_b32 v27, v26 offset:12544\n"
    "v_and_b32 v11, 1, v1\n"
    "v_and_b32 v22, 48, v1\n"
    "v_bfe_u32 v9, v1, 3, 1\n"
    "v_lshl_add_u32 v27, v9, 7, v22\n"
    "v_add_u32 v27, 12288, v27\n"
    "v_and_b32_e64 v9, 7, v1\n"
    "v_mov_b32 v26, 12544\n"
    "v_cmp_eq_u32 vcc, 0, v9\n"
    "s_nop 1\n"
    "v_cndmask_b32 v48, v26, v27, vcc\n"
    "v_cmp_eq_u32 vcc, 1, v9\n"
    "s_nop 1\n"
    "v_cndmask_b32 v49, v26, v27, vcc\n"
    "v_cmp_eq_u32 vcc, 2, v9\n"
    "s_nop 1\n"
    "v_cndmask_b32 v50, v26, v27, vcc\n"
    "v_cmp_eq_u32 vcc, 3, v9\n"
    "s_nop 1\n"
    "v_cndmask_b32 v51, v26, v27, vcc\n"
    "v_cmp_eq_u32 vcc, 4, v9\n"
    "s_nop 1\n"
    "v_cndmask_b32 v52, v26, v27, vcc\n"
    "v_cmp_eq_u32 vcc, 5, v9\n"
    "s_nop 1\n"
    "v_cndmask_b32 v53, v26, v27, vcc\n"
    "v_cmp_eq_u32 vcc, 6, v9\n"
    "s_nop 1\n"
    "v_cndmask_b32 v54, v26, v27, vcc\n"
    "v_cmp_eq_u32 vcc, 7, v9\n"
    "s_nop 1\n"
    "v_cndmask_b32 v55, v26, v27, vcc\n"
    "v_lshrrev_b32_e64 v3, 1, v22\n"
    "v_lshl_add_u32 v3, v11, 7, v3\n"
    "s_lshl_b32 s58, s48, 5\n"
    "v_add_u32_e64 v3, s58, v3\n"
    "s_cmp_eq_u32 s48, 0\n"
    "s_cselect_b32 s58, 0, 0x7f000000\n"
    "v_lshl_add_u32 v9, v1, 4, s58\n"
    "v_mul_u32_u24 v26, 1088, v11\n"
    "s_mov_b32 s58, 13568\n"
    "v_add3_u32 v11, v26, v22, s58\n"
    "v_add_u32_e64 v10, s58, v13\n"
    "s_lshl2_add_u32 s58, s54, s48\n"
    "s_lshl_b32 s58, s58, 10\n"
    "s_add_u32 s16, s58, 163840\n"
    "s_lshl_b32 s58, s54, 4\n"
    "s_add_u32 s58, s58, 851968\n"
    "s_cmp_eq_u32 s48, 0\n"
    "s_cselect_b32 s58, s58, 0x7f000000\n"
    "v_lshl_add_u32 v8, v1, 12, s58\n"
    "s_lshl_b32 s62, s54, 13\n"
    "s_add_u32 s62, s62, 524288\n"
    "s_waitcnt lgkmcnt(0)\n"
    "s_branch .Lk_Lgather\n"
    ".Lk_Lnoprep:\n"
    "s_sleep 17\n"
    ".Lk_Lgather:\n"
    "buffer_load_dwordx4 v[128:131], v13, s[20:23], s66 offen offset:0 sc1\n"
    "buffer_load_dwordx4 v[132:135], v13, s[20:23], s66 offen offset:1024 sc1\n"
    "buffer_load_dwordx4 v[136:139], v13, s[20:23], s66 offen offset:2048 sc1\n"
    "buffer_load_dwordx4 v[140:143], v13, s[20:23], s66 offen offset:3072 sc1\n"
    "buffer_load_dword v160, v61, s[20:23], s64 offen sc1\n"
    "buffer_load_dwordx4 v[144:147], v13, s[20:23], s67 offen offset:0 sc1\n"
    "buffer_load_dwordx4 v[148:151], v13, s[20:23], s67 offen offset:1024 sc1\n"
    "buffer_load_dwordx4 v[152:155], v13, s[20:23], s67 offen offset:2048 sc1\n"
    "buffer_load_dwordx4 v[156:159], v13, s[20:23], s67 offen offset:3072 sc1\n"
    "buffer_load_dword v161, v61, s[20:23], s65 offen sc1\n"
    "s_setprio 0\n"
    "s_barrier\n"
    "s_setprio 3\n"
    "s_waitcnt vmcnt(9)\n"
    "ds_write_b128 v15, v[128:131] offset:15872\n"
    "v_add3_u32 v14, s10, v128, v129\n"
    "v_add3_u32 v14, v14, v130, v131\n"
    "s_waitcnt vmcnt(8)\n"
    "ds_write_b128 v15, v[132:135] offset:16896\n"
    "v_add3_u32 v14, v14, v132, v133\n"
    "v_add3_u32 v14, v14, v134, v135\n"
    "s_waitcnt vmcnt(7)\n"
    "ds_write_b128 v15, v[136:139] offset:17920\n"
    "v_add3_u32 v14, v14, v136, v137\n"
    "v_add3_u32 v14, v14, v138, v139\n"
    "s_waitcnt vmcnt(6)\n"
    "ds_write_b128 v15, v[140:143] offset:18944\n"
    "v_add3_u32 v14, v14, v140, v141\n"
    "v_add3_u32 v14, v14, v142, v143\n"
    "s_waitcnt vmcnt(5)\n"
    "v_sub_u32 v14, v14, v160\n"
    "s_nop 1\n"
    "v_add_u32_dpp v14, v14, v14 quad_perm:[1,0,3,2] row_mask:0xf bank_mask:0xf\n"
    "s_nop 1\n"
    "v_add_u32_dpp v14, v14, v14 quad_perm:[2,3,0,1] row_mask:0xf bank_mask:0xf\n"
    "v_cmp_ne_u32 vcc, 0, v14\n"
    "s_cbranch_vccnz .Lk_R_o0\n"
    ".Lk_B_o0:\n"
    "s_waitcnt vmcnt(4)\n"
    "ds_write_b128 v15, v[144:147] offset:19968\n"
    "v_add3_u32 v14, s10, v144, v145\n"
    "v_add3_u32 v14, v14, v146, v147\n"
    "s_waitcnt vmcnt(3)\n"
    "ds_write_b128 v15, v[148:151] offset:20992\n"
    "v_add3_u32 v14, v14, v148, v149\n"
    "v_add3_u32 v14, v14, v150, v151\n"
    "s_waitcnt vmcnt(2)\n"
    "ds_write_b128 v15, v[152:155] offset:22016\n"
    "v_add3_u32 v14, v14, v152, v153\n"
    "v_add3_u32 v14, v14, v154, v155\n"
    "s_waitcnt vmcnt(1)\n"
    "ds_write_b128 v15, v[156:159] offset:23040\n"
    "v_add3_u32 v14, v14, v156, v157\n"
    "v_add3_u32 v14, v14, v158, v159\n"
    "s_waitcnt vmcnt(0)\n"
    "v_sub_u32 v14, v14, v161\n"
    "s_nop 1\n"
    "v_add_u32_dpp v14, v14, v14 quad_perm:[1,0,3,2] row_mask:0xf bank_mask:0xf\n"
    "s_nop 1\n"
    "v_add_u32_dpp v14, v14, v14 quad_perm:[2,3,0,1] row_mask:0xf bank_mask:0xf\n"
    "v_cmp_ne_u32 vcc, 0, v14\n"
    "s_cbranch_vccnz .Lk_R_o1\n"
    ".Lk_B_o1:\n"
    "s_setprio 0\n"
    "s_waitcnt lgkmcnt(0)\n"
    "s_barrier\n"
    "s_cmp_lt_i32 s54, 0\n"
    "s_cbranch_scc0 .Lk_Lisffn\n"
    "s_sleep 6\n"
    "s_cmp_eq_u32 s17, 2\n"
    "s_mov_b32 s58, 0x0\n"
    "s_cselect_b32 vcc_lo, 0xffffffff, s58\n"
    "s_mov_b32 s58, 0xffff0000\n"
    "s_cselect_b32 vcc_hi, 0xffffffff, s58\n"
    "v_mov_b32 v14, 0x7f000000\n"
    "v_cndmask_b32 v14, v14, v62, vcc\n"
    "buffer_load_dword v63, v14, s[4:7], 0 offen\n"
    "s_barrier\n"
    "s_branch .Lk_LexitA\n"
    ".Lk_Lisffn:\n"
    "s_add_u32 m0, s98, 147456\n"
    "s_mov_b32 s3, 688128\n"
    "buffer_load_dwordx4 v16, s[36:39], s3 offen lds\n"
    "s_add_u32 m0, s98, 148480\n"
    "s_mov_b32 s3, 688128\n"
    "buffer_load_dwordx4 v16, s[40:43], s3 offen lds\n"
    "s_add_u32 m0, s98, 149504\n"
    "s_mov_b32 s3, 688128\n"
    "buffer_load_dwordx4 v17, s[36:39], s3 offen lds\n"
    "s_add_u32 m0, s98, 150528\n"
    "s_mov_b32 s3, 688128\n"
    "buffer_load_dwordx4 v17, s[40:43], s3 offen lds\n"
    "s_add_u32 m0, s98, 48640\n"
    "s_mov_b32 s3, 917504\n"
    "buffer_load_dwordx4 v16, s[36:39], s3 offen lds\n"
    "s_add_u32 m0, s98, 49664\n"
    "s_mov_b32 s3, 917504\n"
    "buffer_load_dwordx4 v16, s[40:43], s3 offen lds\n"
    "s_add_u32 m0, s98, 50688\n"
    "s_mov_b32 s3, 917504\n"
    "buffer_load_dwordx4 v17, s[36:39], s3 offen lds\n"
    "s_add_u32 m0, s98, 51712\n"
    "s_mov_b32 s3, 917504\n"
    "buffer_load_dwordx4 v17, s[40:43], s3 offen lds\n"
    "s_add_u32 m0, s98, 81920\n"
    "s_mov_b32 s3, 1146880\n"
    "buffer_load_dwordx4 v16, s[36:39], s3 offen lds\n"
    "s_add_u32 m0, s98, 82944\n"
    "s_mov_b32 s3, 1146880\n"
    "buffer_load_dwordx4 v16, s[40:43], s3 offen lds\n"
    "s_add_u32 m0, s98, 83968\n"
    "s_mov_b32 s3, 1146880\n"
    "buffer_load_dwordx4 v17, s[36:39], s3 offen lds\n"
    "s_add_u32 m0, s98, 84992\n"
    "s_mov_b32 s3, 1146880\n"
    "buffer_load_dwordx4 v17, s[40:43], s3 offen lds\n"
    "s_lshl2_add_u32 s58, s54, s48\n"
    "s_lshl_b32 s58, s58, 10\n"
    "s_add_u32 s58, s58, 163840\n"
    "v_not_b32 v24, s55\n"
    "v_lshrrev_b32 v25, 8, v24\n"
    "v_lshrrev_b32 v26, 16, v24\n"
    "v_lshrrev_b32 v27, 24, v24\n"
    "buffer_store_dwordx4 v[24:27], v13, s[20:23], s58 offen sc1\n"
    "v_not_b32_e64 v28, s8\n"
    "s_mov_b64 exec, 0xffff\n"
    "buffer_store_dword v28, v8, s[20:23], 0 offen sc1\n"
    "s_mov_b64 exec, -1\n"
    "s_nop 0\n"
    "ds_read_b64_tr_b16 v[128:129], v6 offset:33280\n"
    "ds_read_b64_tr_b16 v[130:131], v6 offset:33792\n"
    "ds_read_b64_tr_b16 v[132:133], v6 offset:34304\n"
    "ds_read_b64_tr_b16 v[134:135], v6 offset:34816\n"
    "ds_read_b64_tr_b16 v[136:137], v6 offset:41472\n"
    "ds_read_b64_tr_b16 v[138:139], v6 offset:41984\n"
    "ds_read_b64_tr_b16 v[140:141], v6 offset:42496\n"
    "ds_read_b64_tr_b16 v[142:143], v6 offset:43008\n"
    "ds_read_b64_tr_b16 v[144:145], v6 offset:49664\n"
    "ds_read_b64_tr_b16 v[146:147], v6 offset:50176\n"
    "ds_read_b64_tr_b16 v[148:149], v6 offset:50688\n"
    "ds_read_b64_tr_b16 v[150:151], v6 offset:51200\n"
    "ds_read_b64_tr_b16 v[152:153], v6 offset:57856\n"
    "ds_read_b64_tr_b16 v[154:155], v6 offset:58368\n"
    "ds_read_b64_tr_b16 v[156:157], v6 offset:58880\n"
    "ds_read_b64_tr_b16 v[158:159], v6 offset:59392\n"
    "ds_read_b64_tr_b16 v[160:161], v5 offset:49152\n"
    "ds_read_b64_tr_b16 v[162:163], v5 offset:49664\n"
    "ds_read_b64_tr_b16 v[164:165], v5 offset:50176\n"
    "ds_read_b64_tr_b16 v[166:167], v5 offset:50688\n"
    "ds_read_b64_tr_b16 v[168:169], v5 offset:57344\n"
    "ds_read_b64_tr_b16 v[170:171], v5 offset:57856\n"
    "ds_read_b64_tr_b16 v[172:173], v5 offset:58368\n"
    "ds_read_b64_tr_b16 v[174:175], v5 offset:58880\n"
    "s_waitcnt vmcnt(2) lgkmcnt(0)\n"
    "s_barrier\n"
    "s_sleep 4\n"
    "s_nop 0\n"
    "s_add_u32 m0, s98, 98304\n"
    "s_mov_b32 s3, 1376256\n"
    "buffer_load_dwordx4 v16, s[36:39], s3 offen lds\n"
    "s_add_u32 m0, s98, 99328\n"
    "s_mov_b32 s3, 1376256\n"
    "buffer_load_dwordx4 v16, s[40:43], s3 offen lds\n"
    "s_add_u32 m0, s98, 100352\n"
    "s_mov_b32 s3, 1376256\n"
    "buffer_load_dwordx4 v17, s[36:39], s3 offen lds\n"
    "s_add_u32 m0, s98, 101376\n"
    "s_mov_b32 s3, 1376256\n"
    "buffer_load_dwordx4 v17, s[40:43], s3 offen lds\n"
    "s_add_u32 m0, s98, 65024\n"
    "s_mov_b32 s3, 1605632\n"
    "buffer_load_dwordx4 v16, s[36:39], s3 offen lds\n"
    "s_add_u32 m0, s98, 66048\n"
    "s_mov_b32 s3, 1605632\n"
    "buffer_load_dwordx4 v16, s[40:43], s3 offen lds\n"
    "s_add_u32 m0, s98, 67072\n"
    "s_mov_b32 s3, 1605632\n"
    "buffer_load_dwordx4 v17, s[36:39], s3 offen lds\n"
    "s_add_u32 m0, s98, 68096\n"
    "s_mov_b32 s3, 1605632\n"
    "buffer_load_dwordx4 v17, s[40:43], s3 offen lds\n"
    "s_add_u32 m0, s0, 15872\n"
    "s_add_u32 s3, s99, 0\n"
    "s_nop 0\n"
    "buffer_load_dwordx4 v18, s[44:47], s3 offen offset:0 lds\n"
    "buffer_load_dwordx4 v19, s[44:47], s3 offen offset:1024 lds\n"
    "buffer_load_dwordx4 v20, s[44:47], s3 offen offset:2048 lds\n"
    "buffer_load_dwordx4 v21, s[44:47], s3 offen offset:3072 lds\n"
    "s_add_u32 m0, s0, 19968\n"
    "s_add_u32 s3, s99, 4096\n"
    "buffer_load_dwordx4 v18, s[44:47], s3 offen offset:0 lds\n"
    "buffer_load_dwordx4 v19, s[44:47], s3 offen offset:1024 lds\n"
    "buffer_load_dwordx4 v20, s[44:47], s3 offen offset:2048 lds\n"
    "buffer_load_dwordx4 v21, s[44:47], s3 offen offset:3072 lds\n"
    "s_add_u32 m0, s0, 114688\n"
    "s_add_u32 s3, s99, 32768\n"
    "buffer_load_dwordx4 v18, s[44:47], s3 offen offset:0 lds\n"
    "buffer_load_dwordx4 v19, s[44:47], s3 offen offset:1024 lds\n"
    "buffer_load_dwordx4 v20, s[44:47], s3 offen offset:2048 lds\n"
    "buffer_load_dwordx4 v21, s[44:47], s3 offen offset:3072 lds\n"
    "s_add_u32 m0, s0, 118784\n"
    "s_add_u32 s3, s99, 36864\n"
    "buffer_load_dwordx4 v18, s[44:47], s3 offen offset:0 lds\n"
    "buffer_load_dwordx4 v19, s[44:47], s3 offen offset:1024 lds\n"
    "buffer_load_dwordx4 v20, s[44:47], s3 offen offset:2048 lds\n"
    "buffer_load_dwordx4 v21, s[44:47], s3 offen offset:3072 lds\n"
    "ds_read_b64_tr_b16 v[176:177], v7 offset:512\n"
    "ds_read_b64_tr_b16 v[178:179], v7 offset:1024\n"
    "ds_read_b64_tr_b16 v[180:181], v7 offset:1536\n"
    "ds_read_b64_tr_b16 v[182:183], v7 offset:2048\n"
    "ds_read_b64_tr_b16 v[184:185], v7 offset:8704\n"
    "ds_read_b64_tr_b16 v[186:187], v7 offset:9216\n"
    "ds_read_b64_tr_b16 v[188:189], v7 offset:9728\n"
    "ds_read_b64_tr_b16 v[190:191], v7 offset:10240\n"
    "ds_read_b64_tr_b16 v[192:193], v5 offset:32768\n"
    "ds_read_b64_tr_b16 v[194:195], v5 offset:33280\n"
    "ds_read_b64_tr_b16 v[196:197], v5 offset:33792\n"
    "ds_read_b64_tr_b16 v[198:199], v5 offset:34304\n"
    "ds_read_b64_tr_b16 v[200:201], v5 offset:40960\n"
    "ds_read_b64_tr_b16 v[202:203], v5 offset:41472\n"
    "ds_read_b64_tr_b16 v[204:205], v5 offset:41984\n"
    "ds_read_b64_tr_b16 v[206:207], v5 offset:42496\n"
    "ds_read_b64_tr_b16 v[208:209], v6 offset:512\n"
    "ds_read_b64_tr_b16 v[210:211], v6 offset:1024\n"
    "ds_read_b64_tr_b16 v[212:213], v6 offset:1536\n"
    "ds_read_b64_tr_b16 v[214:215], v6 offset:2048\n"
    "ds_read_b64_tr_b16 v[216:217], v6 offset:8704\n"
    "ds_read_b64_tr_b16 v[218:219], v6 offset:9216\n"
    "ds_read_b64_tr_b16 v[220:221], v6 offset:9728\n"
    "ds_read_b64_tr_b16 v[222:223], v6 offset:10240\n"
    "s_waitcnt vmcnt(0) lgkmcnt(0)\n"
    "v_mov_b32_e64 v14, 1\n"
    "s_mov_b64 exec, 1\n"
    "ds_add_u32 v14, v14 offset:13263\n"
    "s_mov_b64 exec, -1\n"
    "s_waitcnt lgkmcnt(0)\n"
    "s_mov_b32 s1, 8388608\n"
    "v_mov_b32_e64 v14, 0\n"
    ".Lk_Lw8_4:\n"
    "ds_read_b32 v56, v14 offset:13264\n"
    "s_sub_u32 s1, s1, 1\n"
    "s_cbranch_scc1 .Lk_Lx8_4\n"
    "s_waitcnt lgkmcnt(0)\n"
    "v_readfirstlane_b32 s60, v56\n"
    "s_waitcnt lgkmcnt(0)\n"
    "s_cmp_lt_u32 s60, 4\n"
    "s_cbranch_scc1 .Lk_Lw8_4\n"
    ".Lk_Lx8_4:\n"
    "s_waitcnt lgkmcnt(0)\n"
    "buffer_load_dwordx3 v[16:18], v9, s[20:23], s62 offen sc1\n"
    "ds_read_b64_tr_b16 v[224:225], v6 offset:16896\n"
    "ds_read_b64_tr_b16 v[226:227], v6 offset:17408\n"
    "ds_read_b64_tr_b16 v[228:229], v6 offset:17920\n"
    "ds_read_b64_tr_b16 v[230:231], v6 offset:18432\n"
    "ds_read_b64_tr_b16 v[232:233], v6 offset:25088\n"
    "ds_read_b64_tr_b16 v[234:235], v6 offset:25600\n"
    "ds_read_b64_tr_b16 v[236:237], v6 offset:26112\n"
    "ds_read_b64_tr_b16 v[238:239], v6 offset:26624\n"
    "ds_read_b64_tr_b16 v[240:241], v5 offset:49152\n"
    "ds_read_b64_tr_b16 v[242:243], v5 offset:49664\n"
    "ds_read_b64_tr_b16 v[244:245], v5 offset:50176\n"
    "ds_read_b64_tr_b16 v[246:247], v5 offset:50688\n"
    "ds_read_b64_tr_b16 v[248:249], v5 offset:57344\n"
    "ds_read_b64_tr_b16 v[250:251], v5 offset:57856\n"
    "ds_read_b64_tr_b16 v[252:253], v5 offset:58368\n"
    "ds_read_b64_tr_b16 v[254:255], v5 offset:58880\n"
    "ds_read_b64_tr_b16 v[64:65], v40 offset:0\n"
    "ds_read_b64_tr_b16 v[66:67], v40 offset:4096\n"
    "ds_read_b64_tr_b16 v[72:73], v41 offset:0\n"
    "ds_read_b64_tr_b16 v[74:75], v41 offset:4096\n"
    "ds_read_b64_tr_b16 v[80:81], v42 offset:0\n"
    "ds_read_b64_tr_b16 v[82:83], v42 offset:4096\n"
    "ds_read_b64_tr_b16 v[88:89], v43 offset:0\n"
    "ds_read_b64_tr_b16 v[90:91], v43 offset:4096\n"
    "ds_read_b64_tr_b16 v[96:97], v44 offset:0\n"
    "ds_read_b64_tr_b16 v[98:99], v44 offset:4096\n"
    "ds_read_b64_tr_b16 v[104:105], v45 offset:0\n"
    "ds_read_b64_tr_b16 v[106:107], v45 offset:4096\n"
    "ds_read_b64_tr_b16 v[112:113], v46 offset:0\n"
    "ds_read_b64_tr_b16 v[114:115], v46 offset:4096\n"
    "ds_read_b64_tr_b16 v[120:121], v47 offset:0\n"
    "ds_read_b64_tr_b16 v[122:123], v47 offset:4096\n"
    "v_add_u32 v40, 98816, v40\n"
    "v_add_u32 v41, 98816, v41\n"
    "v_add_u32 v42, 98816, v42\n"
    "v_add_u32 v43, 98816, v43\n"
    "v_add_u32 v44, 98816, v44\n"
    "v_add_u32 v45, 98816, v45\n"
    "v_add_u32 v46, 98816, v46\n"
    "v_add_u32 v47, 98816, v47\n"
    "ds_read_b64_tr_b16 v[68:69], v40 offset:0\n"
    "ds_read_b64_tr_b16 v[70:71], v40 offset:4096\n"
    "ds_read_b64_tr_b16 v[76:77], v41 offset:0\n"
    "ds_read_b64_tr_b16 v[78:79], v41 offset:4096\n"
    "ds_read_b64_tr_b16 v[84:85], v42 offset:0\n"
    "ds_read_b64_tr_b16 v[86:87], v42 offset:4096\n"
    "ds_read_b64_tr_b16 v[92:93], v43 offset:0\n"
    "ds_read_b64_tr_b16 v[94:95], v43 offset:4096\n"
    "ds_read_b64_tr_b16 v[100:101], v44 offset:0\n"
    "ds_read_b64_tr_b16 v[102:103], v44 offset:4096\n"
    "ds_read_b64_tr_b16 v[108:109], v45 offset:0\n"
    "ds_read_b64_tr_b16 v[110:111], v45 offset:4096\n"
    "ds_read_b64_tr_b16 v[116:117], v46 offset:0\n"
    "ds_read_b64_tr_b16 v[118:119], v46 offset:4096\n"
    "ds_read_b64_tr_b16 v[124:125], v47 offset:0\n"
    "ds_read_b64_tr_b16 v[126:127], v47 offset:4096\n"
    "s_setprio 3\n"
    "s_cmp_eq_u32 s48, 0\n"
    "v_mov_b32 v12, s8\n"
    "v_mov_b32_e64 v15, 0\n"
    "s_cbranch_scc0 .Lk_Lrows\n"
    "s_mov_b32 s1, 131072\n"
    "s_waitcnt vmcnt(0)\n"
    "v_add3_u32 v14, v16, v17, s8\n"
    "v_cmp_ne_u32 s[12:13], v14, v18\n"
    ".Lk_Lbest:\n"
    "s_nop 1\n"
    "v_cndmask_b32 v36, v16, v15, s[12:13]\n"
    "v_cndmask_b32 v37, v17, v15, s[12:13]\n"
    "s_mov_b64 exec, s[12:13]\n"
    "s_cbranch_execz .Lk_Lnonext\n"
    "buffer_load_dwordx3 v[16:18], v9, s[20:23], s62 offen sc1\n"
    ".Lk_Lnonext:\n"
    "s_mov_b64 exec, -1\n"
    "v_min_u32_dpp v56, v36, v36 quad_perm:[1,0,3,2] row_mask:0xf bank_mask:0xf\n"
    "v_max_u32_dpp v20, v36, v36 quad_perm:[1,0,3,2] row_mask:0xf bank_mask:0xf\n"
    "v_max_u32_dpp v57, v37, v37 quad_perm:[1,0,3,2] row_mask:0xf bank_mask:0xf\n"
    "v_max_u32 v21, v56, v57\n"
    "v_min_u32_dpp v56, v20, v20 quad_perm:[2,3,0,1] row_mask:0xf bank_mask:0xf\n"
    "v_max_u32_dpp v20, v20, v20 quad_perm:[2,3,0,1] row_mask:0xf bank_mask:0xf\n"
    "v_max_u32_dpp v57, v21, v21 quad_perm:[2,3,0,1] row_mask:0xf bank_mask:0xf\n"
    "v_max_u32_e64 v21, v56, v57\n"
    "v_min_u32_dpp v56, v20, v20 row_half_mirror row_mask:0xf bank_mask:0xf\n"
    "v_max_u32_dpp v20, v20, v20 row_half_mirror row_mask:0xf bank_mask:0xf\n"
    "v_max_u32_dpp v57, v21, v21 row_half_mirror row_mask:0xf bank_mask:0xf\n"
    "v_max_u32_e64 v21, v56, v57\n"
    "v_min_u32_dpp v56, v20, v20 row_mirror row_mask:0xf bank_mask:0xf\n"
    "v_max_u32_dpp v20, v20, v20 row_mirror row_mask:0xf bank_mask:0xf\n"
    "v_max_u32_dpp v57, v21, v21 row_mirror row_mask:0xf bank_mask:0xf\n"
    "v_max_u32 v21, v56, v57\n"
    "v_mov_b32 v56, v20\n"
    "v_mov_b32 v57, v21\n"
    "s_nop 0\n"
    "v_permlane16_swap_b32 v20, v56\n"
    "v_permlane16_swap_b32 v21, v57\n"
    "v_min_u32 v58, v20, v56\n"
    "v_max_u32 v20, v20, v56\n"
    "v_max3_u32 v21, v21, v57, v58\n"
    "v_mov_b32 v56, v20\n"
    "v_mov_b32 v57, v21\n"
    "s_nop 0\n"
    "v_permlane32_swap_b32 v20, v56\n"
    "v_permlane32_swap_b32 v21, v57\n"
    "v_min_u32 v58, v20, v56\n"
    "v_max_u32_e64 v20, v20, v56\n"
    "v_max3_u32 v21, v21, v57, v58\n"
    "v_readfirstlane_b32 s64, v20\n"
    "v_readfirstlane_b32 s65, v21\n"
    "s_andn2_b32 s64, 0x7ff, s64\n"
    "s_lshl_b32 s66, s64, 8\n"
    "s_lshl2_add_u32 s64, s66, 8388608\n"
    "s_add_u32 s66, s66, 10485760\n"
    "s_andn2_b32 s65, 0x7ff, s65\n"
    "s_lshl_b32 s67, s65, 8\n"
    "s_lshl2_add_u32 s65, s67, 8388608\n"
    "s_add_u32 s67, s67, 10485760\n"
    "buffer_load_dwordx4 v[24:27], v13, s[20:23], s64 offen sc1\n"
    "buffer_load_dwordx4 v[28:31], v13, s[20:23], s65 offen sc1\n"
    "buffer_load_dword v32, v4, s[20:23], s66 offen sc1\n"
    "buffer_load_dword v33, v4, s[20:23], s67 offen sc1\n"
    "s_cmp_lg_u64 s[12:13], 0\n"
    "s_cbranch_scc1 .Lk_Rkeys\n"
    ".Lk_Bkeys:\n"
    "s_waitcnt vmcnt(3)\n"
    "ds_write_b128 v10, v[24:27] offset:0\n"
    "v_add3_u32 v34, v24, v25, s8\n"
    "v_add3_u32 v34, v34, v26, v27\n"
    "s_waitcnt vmcnt(2)\n"
    "ds_write_b128 v10, v[28:31] offset:1088\n"
    "v_add3_u32 v35, v28, v29, s8\n"
    "v_add3_u32 v35, v35, v30, v31\n"
    "s_waitcnt vmcnt(0)\n"
    "v_cmp_ne_u64 vcc, v[32:33], v[34:35]\n"
    "ds_write_b64 v15, v[20:21] offset:13184\n"
    "s_cbranch_vccnz .Lk_R_cf\n"
    ".Lk_B_cf:\n"
    "s_waitcnt lgkmcnt(0)\n"
    "s_branch .Lk_Lrows2\n"
    ".Lk_Lrows:\n"
    "s_waitcnt vmcnt(0)\n"
    ".Lk_Lrows2:\n"
    "s_barrier\n"
    "ds_read_b128 v[24:27], v11 offset:0\n"
    "ds_read_b128 v[28:31], v11 offset:64\n"
    "ds_read_b128 v[32:35], v11 offset:128\n"
    "ds_read_b128 v[36:39], v11 offset:192\n"
    "ds_read_b128 v[16:19], v11 offset:256\n"
    "s_waitcnt lgkmcnt(4)\n"
    "v_mfma_f32_16x16x32_bf16 v[56:59], v[128:131], v[24:27], 0\n"
    "ds_read_b128 v[20:23], v11 offset:320\n"
    "v_mfma_f32_16x16x32_bf16 v[60:63], v[132:135], v[24:27], 0\n"
    "s_waitcnt lgkmcnt(4)\n"
    "v_mfma_f32_16x16x32_bf16 v[56:59], v[136:139], v[28:31], v[56:59]\n"
    "ds_read_b128 v[24:27], v11 offset:384\n"
    "v_mfma_f32_16x16x32_bf16 v[60:63], v[140:143], v[28:31], v[60:63]\n"
    "s_waitcnt lgkmcnt(4)\n"
    "v_mfma_f32_16x16x32_bf16 v[56:59], v[144:147], v[32:35], v[56:59]\n"
    "ds_read_b128 v[28:31], v11 offset:448\n"
    "v_mfma_f32_16x16x32_bf16 v[60:63], v[148:151], v[32:35], v[60:63]\n"
    "s_waitcnt lgkmcnt(4)\n"
    "v_mfma_f32_16x16x32_bf16 v[56:59], v[152:155], v[36:39], v[56:59]\n"
    "ds_read_b128 v[32:35], v11 offset:512\n"
    "v_mfma_f32_16x16x32_bf16 v[60:63], v[156:159], v[36:39], v[60:63]\n"
    "s_waitcnt lgkmcnt(4)\n"
    "v_mfma_f32_16x16x32_bf16 v[56:59], v[160:163], v[16:19], v[56:59]\n"
    "ds_read_b128 v[36:39], v11 offset:576\n"
    "v_mfma_f32_16x16x32_bf16 v[60:63], v[164:167], v[16:19], v[60:63]\n"
    "s_mov_b64 exec, 0xffff\n"
    "buffer_store_dword v12, v8, s[20:23], 0 offen sc1\n"
    "s_mov_b64 exec, -1\n"
    "s_waitcnt lgkmcnt(4)\n"
    "v_mfma_f32_16x16x32_bf16 v[56:59], v[168:171], v[20:23], v[56:59]\n"
    "ds_read_b128 v[16:19], v11 offset:640\n"
    "v_mfma_f32_16x16x32_bf16 v[60:63], v[172:175], v[20:23], v[60:63]\n"
    "s_waitcnt lgkmcnt(4)\n"
    "v_mfma_f32_16x16x32_bf16 v[56:59], v[176:179], v[24:27], v[56:59]\n"
    "ds_read_b128 v[20:23], v11 offset:704\n"
    "v_mfma_f32_16x16x32_bf16 v[60:63], v[180:183], v[24:27], v[60:63]\n"
    "s_waitcnt lgkmcnt(4)\n"
    "v_mfma_f32_16x16x32_bf16 v[56:59], v[184:187], v[28:31], v[56:59]\n"
    "ds_read_b128 v[24:27], v11 offset:768\n"
    "v_mfma_f32_16x16x32_bf16 v[60:63], v[188:191], v[28:31], v[60:63]\n"
    "s_waitcnt lgkmcnt(4)\n"
    "v_mfma_f32_16x16x32_bf16 v[56:59], v[192:195], v[32:35], v[56:59]\n"
    "ds_read_b128 v[28:31], v11 offset:832\n"
    "v_mfma_f32_16x16x32_bf16 v[60:63], v[196:199], v[32:35], v[60:63]\n"
    "s_waitcnt lgkmcnt(4)\n"
    "v_mfma_f32_16x16x32_bf16 v[56:59], v[200:203], v[36:39], v[56:59]\n"
    "ds_read_b128 v[32:35], v11 offset:896\n"
    "v_mfma_f32_16x16x32_bf16 v[60:63], v[204:207], v[36:39], v[60:63]\n"
    "s_waitcnt lgkmcnt(4)\n"
    "v_mfma_f32_16x16x32_bf16 v[56:59], v[208:211], v[16:19], v[56:59]\n"
    "ds_read_b128 v[36:39], v11 offset:960\n"
    "v_mfma_f32_16x16x32_bf16 v[60:63], v[212:215], v[16:19], v[60:63]\n"
    "s_waitcnt lgkmcnt(4)\n"
    "v_mfma_f32_16x16x32_bf16 v[56:59], v[216:219], v[20:23], v[56:59]\n"
    "v_mfma_f32_16x16x32_bf16 v[60:63], v[220:223], v[20:23], v[60:63]\n"
    "s_waitcnt lgkmcnt(3)\n"
    "v_mfma_f32_16x16x32_bf16 v[56:59], v[224:227], v[24:27], v[56:59]\n"
    "v_mfma_f32_16x16x32_bf16 v[60:63], v[228:231], v[24:27], v[60:63]\n"
    "s_waitcnt lgkmcnt(2)\n"
    "v_mfma_f32_16x16x32_bf16 v[56:59], v[232:235], v[28:31], v[56:59]\n"
    "v_mfma_f32_16x16x32_bf16 v[60:63], v[236:239], v[28:31], v[60:63]\n"
    "s_waitcnt lgkmcnt(1)\n"
    "v_mfma_f32_16x16x32_bf16 v[56:59], v[240:243], v[32:35], v[56:59]\n"
    "v_mfma_f32_16x16x32_bf16 v[60:63], v[244:247], v[32:35], v[60:63]\n"
    "s_waitcnt lgkmcnt(0)\n"
    "v_mfma_f32_16x16x32_bf16 v[56:59], v[248:251], v[36:39], v[56:59]\n"
    "v_mfma_f32_16x16x32_bf16 v[60:63], v[252:255], v[36:39], v[60:63]\n"
    "v_bfe_u32 v4, s55, 0, 8\n"
    "v_bfe_u32 v5, s55, 8, 8\n"
    "v_bfe_u32 v6, s55, 16, 8\n"
    "v_bfe_u32 v7, s55, 24, 8\n"
    "s_nop 2\n"
    "v_mul_f32 v16, 0xbfb8aa3b, v56\n"
    "v_mul_f32 v17, 0xbfb8aa3b, v57\n"
    "v_mul_f32 v18, 0xbfb8aa3b, v58\n"
    "v_mul_f32 v19, 0xbfb8aa3b, v59\n"
    "v_exp_f32 v16, v16\n"
    "v_exp_f32 v17, v17\n"
    "v_exp_f32 v18, v18\n"
    "v_exp_f32_e64 v19, v19\n"
    "v_pk_mul_f32 v[56:57], v[56:57], v[60:61]\n"
    "v_pk_mul_f32 v[58:59], v[58:59], v[62:63]\n"
    "v_add_f32 v16, 1.0, v16\n"
    "v_add_f32 v17, 1.0, v17\n"
    "v_add_f32 v18, 1.0, v18\n"
    "v_add_f32 v19, 1.0, v19\n"
    "v_rcp_f32 v16, v16\n"
    "v_rcp_f32 v17, v17\n"
    "v_rcp_f32 v18, v18\n"
    "v_rcp_f32 v19, v19\n"
    "v_pk_mul_f32 v[56:57], v[56:57], v[16:17]\n"
    "v_pk_mul_f32 v[58:59], v[58:59], v[18:19]\n"
    "v_cvt_pk_bf16_f32 v56, v56, v57\n"
    "v_cvt_pk_bf16_f32 v57, v58, v59\n"
    "s_mov_b32 exec_lo, 0x00030003\n"
    "s_mov_b32 exec_hi, 0x00030003\n"
    "ds_write_b64 v3, v[56:57] offset:12288\n"
    "s_mov_b64 exec, -1\n"
    "s_waitcnt lgkmcnt(0)\n"
    "s_barrier\n"
    "ds_read_b128 v[128:131], v48 offset:0\n"
    "ds_read_b128 v[136:139], v49 offset:0\n"
    "ds_read_b128 v[144:147], v50 offset:0\n"
    "ds_read_b128 v[152:155], v51 offset:0\n"
    "ds_read_b128 v[160:163], v52 offset:0\n"
    "ds_read_b128 v[168:171], v53 offset:0\n"
    "s_waitcnt lgkmcnt(5)\n"
    "v_mfma_f32_16x16x32_bf16 v[60:63], v[64:67], v[128:131], 0\n"
    "ds_read_b128 v[176:179], v54 offset:0\n"
    "s_waitcnt lgkmcnt(5)\n"
    "v_mfma_f32_16x16x32_bf16 v[60:63], v[72:75], v[136:139], v[60:63]\n"
    "ds_read_b128 v[184:187], v55 offset:0\n"
    "s_waitcnt lgkmcnt(5)\n"
    "v_mfma_f32_16x16x32_bf16 v[60:63], v[80:83], v[144:147], v[60:63]\n"
    "ds_read_b128 v[132:135], v48 offset:64\n"
    "s_waitcnt lgkmcnt(5)\n"
    "v_mfma_f32_16x16x32_bf16 v[60:63], v[88:91], v[152:155], v[60:63]\n"
    "ds_read_b128 v[140:143], v49 offset:64\n"
    "s_waitcnt lgkmcnt(5)\n"
    "v_mfma_f32_16x16x32_bf16 v[60:63], v[96:99], v[160:163], v[60:63]\n"
    "ds_read_b128 v[148:151], v50 offset:64\n"
    "s_waitcnt lgkmcnt(5)\n"
    "v_mfma_f32_16x16x32_bf16 v[60:63], v[104:107], v[168:171], v[60:63]\n"
    "ds_read_b128 v[156:159], v51 offset:64\n"
    "s_waitcnt lgkmcnt(5)\n"
    "v_mfma_f32_16x16x32_bf16 v[60:63], v[112:115], v[176:179], v[60:63]\n"
    "ds_read_b128 v[164:167], v52 offset:64\n"
    "s_waitcnt lgkmcnt(5)\n"
    "v_mfma_f32_16x16x32_bf16 v[60:63], v[120:123], v[184:187], v[60:63]\n"
    "ds_read_b128 v[172:175], v53 offset:64\n"
    "s_waitcnt lgkmcnt(5)\n"
    "v_mfma_f32_16x16x32_bf16 v[60:63], v[68:71], v[132:135], v[60:63]\n"
    "ds_read_b128 v[180:183], v54 offset:64\n"
    "s_waitcnt lgkmcnt(5)\n"
    "v_mfma_f32_16x16x32_bf16 v[60:63], v[76:79], v[140:143], v[60:63]\n"
    "ds_read_b128 v[188:191], v55 offset:64\n"
    "s_waitcnt lgkmcnt(5)\n"
    "v_mfma_f32_16x16x32_bf16 v[60:63], v[84:87], v[148:151], v[60:63]\n"
    "s_waitcnt lgkmcnt(4)\n"
    "v_mfma_f32_16x16x32_bf16 v[60:63], v[92:95], v[156:159], v[60:63]\n"
    "s_waitcnt lgkmcnt(3)\n"
    "v_mfma_f32_16x16x32_bf16 v[60:63], v[100:103], v[164:167], v[60:63]\n"
    "s_waitcnt lgkmcnt(2)\n"
    "v_mfma_f32_16x16x32_bf16 v[60:63], v[108:111], v[172:175], v[60:63]\n"
    "s_waitcnt lgkmcnt(1)\n"
    "v_mfma_f32_16x16x32_bf16 v[60:63], v[116:119], v[180:183], v[60:63]\n"
    "s_waitcnt lgkmcnt(0)\n"
    "v_mfma_f32_16x16x32_bf16 v[60:63], v[124:127], v[188:191], v[60:63]\n"
    "s_nop 7\n"
    "v_and_or_b32 v60, v60, s11, v4\n"
    "v_and_or_b32 v61, v61, s11, v5\n"
    "v_and_or_b32 v62, v62, s11, v6\n"
    "v_and_or_b32 v63, v63, s11, v7\n"
    "buffer_store_dwordx4 v[60:63], v13, s[20:23], s16 offen sc1\n"
    "s_add_u32 s58, s2, s48\n"
    "s_cmp_eq_u32 s58, 0\n"
    "s_cselect_b32 s58, 0, 0x7f000000\n"
    "v_mov_b32 v14, s58\n"
    "v_mov_b32_e64 v15, s55\n"
    "s_mov_b64 exec, 1\n"
    "buffer_store_dword v15, v14, s[20:23], 0 offen sc1\n"
    ".Lk_LexitA:\n"
    "s_endpgm\n"
    ".Lk_R_mod:\n"
    "s_waitcnt vmcnt(0) lgkmcnt(0)\n"
    "s_mov_b32 s1, 131072\n"
    ".Lk_RL_mod:\n"
    "s_mov_b32 s60, 16384\n"
    "buffer_load_dwordx4 v[80:83], v65, s[20:23], s60 offen sc1\n"
    "s_mov_b32 s60, 28672\n"
    "buffer_load_dwordx4 v[84:87], v65, s[20:23], s60 offen sc1\n"
    "s_mov_b32 s60, 40960\n"
    "buffer_load_dwordx4 v[88:91], v65, s[20:23], s60 offen sc1\n"
    "s_mov_b32 s60, 53248\n"
    "buffer_load_dwordx4 v[92:95], v65, s[20:23], s60 offen sc1\n"
    "s_waitcnt vmcnt(3)\n"
    "v_xor_b32 v80, v52, v80\n"
    "v_xor_b32 v81, v53, v81\n"
    "v_xor_b32 v82, v54, v82\n"
    "v_xor_b32 v83, v55, v83\n"
    "v_or_b32 v14, v80, v81\n"
    "v_or3_b32 v14, v14, v82, v83\n"
    "s_waitcnt vmcnt(2)\n"
    "v_xor_b32 v84, v52, v84\n"
    "v_xor_b32 v85, v53, v85\n"
    "v_xor_b32 v86, v54, v86\n"
    "v_xor_b32_e64 v87, v55, v87\n"
    "v_or3_b32 v14, v14, v84, v85\n"
    "v_or3_b32 v14, v14, v86, v87\n"
    "v_pk_add_f32 v[70:71], v[80:81], v[84:85]\n"
    "v_pk_add_f32 v[72:73], v[82:83], v[86:87]\n"
    "s_waitcnt vmcnt(1)\n"
    "v_xor_b32 v88, v52, v88\n"
    "v_xor_b32 v89, v53, v89\n"
    "v_xor_b32 v90, v54, v90\n"
    "v_xor_b32_e64 v91, v55, v91\n"
    "v_or3_b32 v14, v14, v88, v89\n"
    "v_or3_b32 v14, v14, v90, v91\n"
    "v_pk_add_f32 v[70:71], v[70:71], v[88:89]\n"
    "v_pk_add_f32 v[72:73], v[72:73], v[90:91]\n"
    "s_waitcnt vmcnt(0)\n"
    "v_xor_b32 v92, v52, v92\n"
    "v_xor_b32 v93, v53, v93\n"
    "v_xor_b32 v94, v54, v94\n"
    "v_xor_b32_e64 v95, v55, v95\n"
    "v_or3_b32 v14, v14, v92, v93\n"
    "v_or3_b32 v14, v14, v94, v95\n"
    "v_pk_add_f32 v[70:71], v[70:71], v[92:93]\n"
    "v_pk_add_f32 v[72:73], v[72:73], v[94:95]\n"
    "v_and_b32 v14, 0xff, v14\n"
    "v_cmp_ne_u32 vcc, 0, v14\n"
    "s_nop 0\n"
    "v_cvt_pk_bf16_f32 v70, 0, v70\n"
    "v_cvt_pk_bf16_f32 v71, 0, v71\n"
    "v_cvt_pk_bf16_f32 v72, 0, v72\n"
    "v_cvt_pk_bf16_f32 v73, 0, v73\n"
    "v_pk_add_f32 v[70:71], v[70:71], v[76:77] op_sel_hi:[1,0]\n"
    "v_pk_add_f32 v[72:73], v[72:73], v[76:77] op_sel_hi:[1,0]\n"
    "v_cvt_pk_bf16_f32 v70, 0, v70\n"
    "v_cvt_pk_bf16_f32 v71, 0, v71\n"
    "v_cvt_pk_bf16_f32 v72, 0, v72\n"
    "v_cvt_pk_bf16_f32 v73, 0, v73\n"
    "ds_write_b128 v66, v[70:73]\n"
    "s_waitcnt vmcnt(0) lgkmcnt(0)\n"
    "s_cbranch_vccz .Lk_B_mod\n"
    "s_sub_u32 s1, s1, 1\n"
    "s_cbranch_scc0 .Lk_RL_mod\n"
    "s_branch .Lk_B_mod\n"
    ".Lk_R_laten:\n"
    "s_waitcnt vmcnt(0) lgkmcnt(0)\n"
    "s_mov_b32 s1, 131072\n"
    ".Lk_RL_laten:\n"
    "s_movk_i32 s60, 12288\n"
    "buffer_load_dwordx4 v[68:71], v50, s[20:23], 0 offen sc1\n"
    "buffer_load_dwordx4 v[72:75], v50, s[20:23], s60 offen sc1\n"
    "s_waitcnt vmcnt(1)\n"
    "v_xor_b32 v68, v52, v68\n"
    "v_xor_b32 v69, v53, v69\n"
    "v_xor_b32 v70, v54, v70\n"
    "v_xor_b32 v71, v55, v71\n"
    "s_waitcnt vmcnt(0)\n"
    "v_xor_b32 v72, v52, v72\n"
    "v_xor_b32 v73, v53, v73\n"
    "v_xor_b32 v74, v54, v74\n"
    "v_xor_b32_e64 v75, v55, v75\n"
    "v_or3_b32 v14, v68, v69, v70\n"
    "v_or3_b32 v14, v14, v71, v72\n"
    "v_or3_b32 v14, v14, v73, v74\n"
    "v_or_b32_e64 v14, v14, v75\n"
    "v_and_b32 v14, 0xff, v14\n"
    "v_cmp_ne_u32 vcc, 0, v14\n"
    "s_waitcnt vmcnt(0) lgkmcnt(0)\n"
    "s_cbranch_vccz .Lk_B_laten\n"
    "s_sub_u32 s1, s1, 1\n"
    "s_cbranch_scc0 .Lk_RL_laten\n"
    "s_branch .Lk_B_laten\n"
    ".Lk_R_stn:\n"
    "s_waitcnt vmcnt(0) lgkmcnt(0)\n"
    "s_mov_b32 s1, 131072\n"
    ".Lk_RL_stn:\n"
    "s_mov_b64 exec, vcc\n"
    "buffer_load_dwordx4 v[128:131], v15, s[20:23], s62 offen offset:0 sc1\n"
    "buffer_load_dwordx4 v[132:135], v15, s[20:23], s62 offen offset:512 sc1\n"
    "s_waitcnt vmcnt(1)\n"
    "v_xor_b32 v128, v52, v128\n"
    "v_xor_b32 v129, v53, v129\n"
    "v_xor_b32 v130, v54, v130\n"
    "v_xor_b32 v131, v55, v131\n"
    "v_or_b32 v14, v128, v129\n"
    "v_or3_b32 v14, v14, v130, v131\n"
    "s_waitcnt vmcnt(0)\n"
    "v_xor_b32 v132, v52, v132\n"
    "v_xor_b32 v133, v53, v133\n"
    "v_xor_b32 v134, v54, v134\n"
    "v_xor_b32_e64 v135, v55, v135\n"
    "v_or3_b32 v14, v14, v132, v133\n"
    "v_or3_b32 v14, v14, v134, v135\n"
    "v_and_b32 v14, 0xff, v14\n"
    "v_cmp_ne_u32 vcc, 0, v14\n"
    "s_mov_b64 exec, -1\n"
    "s_waitcnt vmcnt(0) lgkmcnt(0)\n"
    "s_cbranch_vccz .Lk_B_stn\n"
    "s_sub_u32 s1, s1, 1\n"
    "s_cbranch_scc0 .Lk_RL_stn\n"
    "s_branch .Lk_B_stn\n"
    ".Lk_Rffw:\n"
    "v_mov_b32 v4, 0\n"
    "ds_read_b64 v[148:149], v4 offset:13184\n"
    "ds_read_b128 v[80:83], v14 offset:0\n"
    "ds_read_b128 v[84:87], v14 offset:4352\n"
    "s_lshl_b32 s60, s52, 5\n"
    "v_add_u32_e64 v15, s60, v15\n"
    "s_waitcnt lgkmcnt(2)\n"
    "v_bfi_b32 v5, v148, 0, s14\n"
    "v_bfi_b32 v6, v149, 0, s14\n"
    "v_cmp_ne_u32 vcc, v5, v15\n"
    "v_cmp_ne_u32 s[60:61], v6, v15\n"
    "v_add_u32 v15, 16, v15\n"
    "s_and_b64 s[64:65], vcc, s[60:61]\n"
    "v_cmp_ne_u32 vcc, v5, v15\n"
    "v_cmp_ne_u32 s[60:61], v6, v15\n"
    "v_add_u32_e64 v15, 16, v15\n"
    "s_and_b64 s[66:67], vcc, s[60:61]\n"
    "s_waitcnt lgkmcnt(0)\n"
    "s_barrier\n"
    "s_mov_b64 exec, s[64:65]\n"
    "buffer_store_dwordx4 v[80:83], v3, s[32:35], s58 offen sc1\n"
    "s_mov_b64 exec, s[66:67]\n"
    "buffer_store_dwordx4 v[84:87], v3, s[32:35], s59 offen sc1\n"
    "s_mov_b64 exec, -1\n"
    "s_branch .Lk_Bffw\n"
    ".Lk_R_kb:\n"
    "s_waitcnt vmcnt(0) lgkmcnt(0)\n"
    "s_mov_b32 s1, 131072\n"
    ".Lk_RL_kb:\n"
    "s_mov_b64 exec, vcc\n"
    "buffer_load_dwordx3 v[144:146], v8, s[20:23], s62 offen sc1\n"
    "s_waitcnt vmcnt(0)\n"
    "v_add3_u32 v14, v144, v145, s8\n"
    "v_cmp_ne_u32 vcc, v14, v146\n"
    "s_mov_b64 exec, -1\n"
    "s_waitcnt vmcnt(0) lgkmcnt(0)\n"
    "s_cbranch_vccz .Lk_B_kb\n"
    "s_sub_u32 s1, s1, 1\n"
    "s_cbranch_scc0 .Lk_RL_kb\n"
    "s_branch .Lk_B_kb\n"
    ".Lk_R_h:\n"
    "s_waitcnt vmcnt(0) lgkmcnt(0)\n"
    "s_mov_b32 s1, 131072\n"
    ".Lk_RL_h:\n"
    "s_mov_b64 exec, vcc\n"
    "v_mov_b32 v14, 0\n"
    "buffer_atomic_add v14, v15, s[20:23], s58 offen sc0 sc1\n"
    "s_waitcnt vmcnt(0)\n"
    "v_cmp_ne_u32 vcc, s8, v14\n"
    "s_mov_b64 exec, -1\n"
    "s_waitcnt vmcnt(0) lgkmcnt(0)\n"
    "s_cbranch_vccz .Lk_B_h\n"
    "s_sub_u32 s1, s1, 1\n"
    "s_cbranch_scc0 .Lk_RL_h\n"
    "s_branch .Lk_B_h\n"
    ".Lk_R_f0:\n"
    "s_waitcnt vmcnt(0) lgkmcnt(0)\n"
    "s_mov_b32 s1, 131072\n"
    ".Lk_RL_f0:\n"
    "s_mov_b64 exec, vcc\n"
    "buffer_load_dwordx4 v[128:131], v8, s[20:23], 0 offen offset:0 sc1\n"
    "buffer_load_dwordx4 v[132:135], v8, s[20:23], s62 offen offset:0 sc1\n"
    "buffer_load_dwordx4 v[136:139], v8, s[20:23], s63 offen offset:0 sc1\n"
    "buffer_load_dwordx4 v[140:143], v9, s[20:23], 0 offen offset:0 sc1\n"
    "s_waitcnt vmcnt(3)\n"
    "v_xor_b32 v128, v52, v128\n"
    "v_xor_b32 v129, v53, v129\n"
    "v_xor_b32 v130, v54, v130\n"
    "v_xor_b32 v131, v55, v131\n"
    "v_or_b32 v14, v128, v129\n"
    "v_or3_b32 v14, v14, v130, v131\n"
    "s_waitcnt vmcnt(2)\n"
    "v_xor_b32 v132, v52, v132\n"
    "v_xor_b32 v133, v53, v133\n"
    "v_xor_b32 v134, v54, v134\n"
    "v_xor_b32_e64 v135, v55, v135\n"
    "v_or3_b32 v14, v14, v132, v133\n"
    "v_or3_b32 v14, v14, v134, v135\n"
    "s_waitcnt vmcnt(1)\n"
    "v_xor_b32 v136, v52, v136\n"
    "v_xor_b32 v137, v53, v137\n"
    "v_xor_b32 v138, v54, v138\n"
    "v_xor_b32_e64 v139, v55, v139\n"
    "v_or3_b32 v14, v14, v136, v137\n"
    "v_or3_b32 v14, v14, v138, v139\n"
    "s_waitcnt vmcnt(0)\n"
    "v_xor_b32 v140, v52, v140\n"
    "v_xor_b32 v141, v53, v141\n"
    "v_xor_b32 v142, v54, v142\n"
    "v_xor_b32 v143, v55, v143\n"
    "v_and_b32 v140, v89, v140\n"
    "v_and_b32 v141, v89, v141\n"
    "v_and_b32 v142, v89, v142\n"
    "v_and_b32_e64 v143, v89, v143\n"
    "v_or3_b32 v14, v14, v140, v141\n"
    "v_or3_b32 v14, v14, v142, v143\n"
    "v_and_b32 v14, 0xff, v14\n"
    "v_cmp_ne_u32 vcc, 0, v14\n"
    "s_mov_b64 exec, -1\n"
    "s_waitcnt vmcnt(0) lgkmcnt(0)\n"
    "s_cbranch_vccz .Lk_B_f0\n"
    "s_sub_u32 s1, s1, 1\n"
    "s_cbranch_scc0 .Lk_RL_f0\n"
    "s_branch .Lk_B_f0\n"
    ".Lk_R_f1:\n"
    "s_waitcnt vmcnt(0) lgkmcnt(0)\n"
    "s_mov_b32 s1, 131072\n"
    ".Lk_RL_f1:\n"
    "s_mov_b64 exec, vcc\n"
    "buffer_load_dwordx4 v[128:131], v8, s[20:23], 0 offen offset:128 sc1\n"
    "buffer_load_dwordx4 v[132:135], v8, s[20:23], s62 offen offset:128 sc1\n"
    "buffer_load_dwordx4 v[136:139], v8, s[20:23], s63 offen offset:128 sc1\n"
    "buffer_load_dwordx4 v[140:143], v9, s[20:23], 0 offen offset:128 sc1\n"
    "s_waitcnt vmcnt(3)\n"
    "v_xor_b32 v128, v52, v128\n"
    "v_xor_b32 v129, v53, v129\n"
    "v_xor_b32 v130, v54, v130\n"
    "v_xor_b32 v131, v55, v131\n"
    "v_or_b32_e64 v14, v128, v129\n"
    "v_or3_b32 v14, v14, v130, v131\n"
    "s_waitcnt vmcnt(2)\n"
    "v_xor_b32 v132, v52, v132\n"
    "v_xor_b32 v133, v53, v133\n"
    "v_xor_b32 v134, v54, v134\n"
    "v_xor_b32_e64 v135, v55, v135\n"
    "v_or3_b32 v14, v14, v132, v133\n"
    "v_or3_b32 v14, v14, v134, v135\n"
    "s_waitcnt vmcnt(1)\n"
    "v_xor_b32 v136, v52, v136\n"
    "v_xor_b32 v137, v53, v137\n"
    "v_xor_b32 v138, v54, v138\n"
    "v_xor_b32_e64 v139, v55, v139\n"
    "v_or3_b32 v14, v14, v136, v137\n"
    "v_or3_b32 v14, v14, v138, v139\n"
    "s_waitcnt vmcnt(0)\n"
    "v_xor_b32 v140, v52, v140\n"
    "v_xor_b32 v141, v53, v141\n"
    "v_xor_b32 v142, v54, v142\n"
    "v_xor_b32 v143, v55, v143\n"
    "v_and_b32 v140, v89, v140\n"
    "v_and_b32 v141, v89, v141\n"
    "v_and_b32 v142, v89, v142\n"
    "v_and_b32_e64 v143, v89, v143\n"
    "v_or3_b32 v14, v14, v140, v141\n"
    "v_or3_b32 v14, v14, v142, v143\n"
    "v_and_b32 v14, 0xff, v14\n"
    "v_cmp_ne_u32 vcc, 0, v14\n"
    "s_mov_b64 exec, -1\n"
    "s_waitcnt vmcnt(0) lgkmcnt(0)\n"
    "s_cbranch_vccz .Lk_B_f1\n"
    "s_sub_u32 s1, s1, 1\n"
    "s_cbranch_scc0 .Lk_RL_f1\n"
    "s_branch .Lk_B_f1\n"
    ".Lk_R_kv0:\n"
    "s_cmp_gt_u32 s49, 0\n"
    "s_cbranch_scc0 .Lk_B_kv0\n"
    "s_waitcnt vmcnt(0) lgkmcnt(0)\n"
    "s_mov_b32 s1, 131072\n"
    ".Lk_RL_kv0:\n"
    "s_sub_u32 s61, s68, 4096\n"
    "buffer_load_dwordx4 v[128:131], v13, s[20:23], s61 offen offset:2048 sc1\n"
    "buffer_load_dwordx4 v[160:163], v13, s[20:23], s61 offen offset:3072 sc1\n"
    "buffer_load_dwordx4 v[132:135], v13, s[20:23], s61 offen offset:0 sc1\n"
    "buffer_load_dwordx4 v[168:171], v13, s[20:23], s61 offen offset:1024 sc1\n"
    "buffer_load_dword v56, v4, s[20:23], s9 offen offset:768 sc1\n"
    "s_waitcnt vmcnt(4)\n"
    "v_add3_u32 v14, s8, v128, v129\n"
    "v_add3_u32 v14, v14, v130, v131\n"
    "s_waitcnt vmcnt(3)\n"
    "v_add3_u32 v14, v14, v160, v161\n"
    "v_add3_u32 v14, v14, v162, v163\n"
    "s_waitcnt vmcnt(2)\n"
    "v_add3_u32 v14, v14, v132, v133\n"
    "v_add3_u32 v14, v14, v134, v135\n"
    "s_waitcnt vmcnt(1)\n"
    "v_add3_u32 v14, v14, v168, v169\n"
    "v_add3_u32 v14, v14, v170, v171\n"
    "s_waitcnt vmcnt(0)\n"
    "v_cmp_ne_u32 vcc, v14, v56\n"
    "s_waitcnt vmcnt(0) lgkmcnt(0)\n"
    "s_cbranch_vccz .Lk_B_kv0\n"
    "s_sub_u32 s1, s1, 1\n"
    "s_cbranch_scc0 .Lk_RL_kv0\n"
    "s_branch .Lk_B_kv0\n"
    ".Lk_X_0:\n"
    "v_mfma_f32_32x32x16_bf16 v[240:255], v[36:39], v[116:119], v[240:255]\n"
    "s_mov_b64 exec, 1\n"
    "ds_add_u32 v15, v15 offset:13259\n"
    "s_mov_b64 exec, -1\n"
    "s_mov_b64 exec, 1\n"
    "ds_add_u32 v15, v15 offset:13267\n"
    "s_mov_b64 exec, -1\n"
    "s_waitcnt vmcnt(0)\n"
    "s_branch .Lk_Latt_end\n"
    ".Lk_R_kv1:\n"
    "s_cmp_gt_u32 s49, 1\n"
    "s_cbranch_scc0 .Lk_B_kv1\n"
    "s_waitcnt vmcnt(0) lgkmcnt(0)\n"
    "s_mov_b32 s1, 131072\n"
    ".Lk_RL_kv1:\n"
    "s_sub_u32 s61, s68, 8192\n"
    "buffer_load_dwordx4 v[136:139], v13, s[20:23], s61 offen offset:2048 sc1\n"
    "buffer_load_dwordx4 v[176:179], v13, s[20:23], s61 offen offset:3072 sc1\n"
    "buffer_load_dwordx4 v[140:143], v13, s[20:23], s61 offen offset:0 sc1\n"
    "buffer_load_dwordx4 v[184:187], v13, s[20:23], s61 offen offset:1024 sc1\n"
    "buffer_load_dword v57, v4, s[20:23], s9 offen offset:512 sc1\n"
    "s_waitcnt vmcnt(4)\n"
    "v_add3_u32 v14, s8, v136, v137\n"
    "v_add3_u32 v14, v14, v138, v139\n"
    "s_waitcnt vmcnt(3)\n"
    "v_add3_u32 v14, v14, v176, v177\n"
    "v_add3_u32 v14, v14, v178, v179\n"
    "s_waitcnt vmcnt(2)\n"
    "v_add3_u32 v14, v14, v140, v141\n"
    "v_add3_u32 v14, v14, v142, v143\n"
    "s_waitcnt vmcnt(1)\n"
    "v_add3_u32 v14, v14, v184, v185\n"
    "v_add3_u32 v14, v14, v186, v187\n"
    "s_waitcnt vmcnt(0)\n"
    "v_cmp_ne_u32 vcc, v14, v57\n"
    "s_waitcnt vmcnt(0) lgkmcnt(0)\n"
    "s_cbranch_vccz .Lk_B_kv1\n"
    "s_sub_u32 s1, s1, 1\n"
    "s_cbranch_scc0 .Lk_RL_kv1\n"
    "s_branch .Lk_B_kv1\n"
    ".Lk_X_1:\n"
    "v_mfma_f32_32x32x16_bf16 v[240:255], v[172:175], v[116:119], v[240:255]\n"
    "s_waitcnt vmcnt(0)\n"
    "s_nop 1\n"
    "s_branch .Lk_Latt_end\n"
    ".Lk_R_kv2:\n"
    "s_cmp_gt_u32 s49, 2\n"
    "s_cbranch_scc0 .Lk_B_kv2\n"
    "s_waitcnt vmcnt(0) lgkmcnt(0)\n"
    "s_mov_b32 s1, 131072\n"
    ".Lk_RL_kv2:\n"
    "s_sub_u32 s61, s68, 12288\n"
    "buffer_load_dwordx4 v[144:147], v13, s[20:23], s61 offen offset:2048 sc1\n"
    "buffer_load_dwordx4 v[192:195], v13, s[20:23], s61 offen offset:3072 sc1\n"
    "buffer_load_dwordx4 v[148:151], v13, s[20:23], s61 offen offset:0 sc1\n"
    "buffer_load_dwordx4 v[200:203], v13, s[20:23], s61 offen offset:1024 sc1\n"
    "buffer_load_dword v58, v4, s[20:23], s9 offen offset:256 sc1\n"
    "s_waitcnt vmcnt(4)\n"
    "v_add3_u32 v14, s8, v144, v145\n"
    "v_add3_u32 v14, v14, v146, v147\n"
    "s_waitcnt vmcnt(3)\n"
    "v_add3_u32 v14, v14, v192, v193\n"
    "v_add3_u32 v14, v14, v194, v195\n"
    "s_waitcnt vmcnt(2)\n"
    "v_add3_u32 v14, v14, v148, v149\n"
    "v_add3_u32 v14, v14, v150, v151\n"
    "s_waitcnt vmcnt(1)\n"
    "v_add3_u32 v14, v14, v200, v201\n"
    "v_add3_u32 v14, v14, v202, v203\n"
    "s_waitcnt vmcnt(0)\n"
    "v_cmp_ne_u32 vcc, v14, v58\n"
    "s_waitcnt vmcnt(0) lgkmcnt(0)\n"
    "s_cbranch_vccz .Lk_B_kv2\n"
    "s_sub_u32 s1, s1, 1\n"
    "s_cbranch_scc0 .Lk_RL_kv2\n"
    "s_branch .Lk_B_kv2\n"
    ".Lk_X_2:\n"
    "v_mfma_f32_32x32x16_bf16 v[240:255], v[188:191], v[116:119], v[240:255]\n"
    "s_waitcnt vmcnt(0)\n"
    "s_nop 1\n"
    "s_branch .Lk_Latt_end\n"
    ".Lk_R_kv3:\n"
    "s_cmp_gt_u32 s49, 3\n"
    "s_cbranch_scc0 .Lk_B_kv3\n"
    "s_waitcnt vmcnt(0) lgkmcnt(0)\n"
    "s_mov_b32 s1, 131072\n"
    ".Lk_RL_kv3:\n"
    "s_sub_u32 s61, s68, 16384\n"
    "buffer_load_dwordx4 v[152:155], v13, s[20:23], s61 offen offset:2048 sc1\n"
    "buffer_load_dwordx4 v[208:211], v13, s[20:23], s61 offen offset:3072 sc1\n"
    "buffer_load_dwordx4 v[156:159], v13, s[20:23], s61 offen offset:0 sc1\n"
    "buffer_load_dwordx4 v[216:219], v13, s[20:23], s61 offen offset:1024 sc1\n"
    "buffer_load_dword v59, v4, s[20:23], s9 offen offset:0 sc1\n"
    "s_waitcnt vmcnt(4)\n"
    "v_add3_u32 v14, s8, v152, v153\n"
    "v_add3_u32 v14, v14, v154, v155\n"
    "s_waitcnt vmcnt(3)\n"
    "v_add3_u32 v14, v14, v208, v209\n"
    "v_add3_u32 v14, v14, v210, v211\n"
    "s_waitcnt vmcnt(2)\n"
    "v_add3_u32 v14, v14, v156, v157\n"
    "v_add3_u32 v14, v14, v158, v159\n"
    "s_waitcnt vmcnt(1)\n"
    "v_add3_u32 v14, v14, v216, v217\n"
    "v_add3_u32 v14, v14, v218, v219\n"
    "s_waitcnt vmcnt(0)\n"
    "v_cmp_ne_u32 vcc, v14, v59\n"
    "s_waitcnt vmcnt(0) lgkmcnt(0)\n"
    "s_cbranch_vccz .Lk_B_kv3\n"
    "s_sub_u32 s1, s1, 1\n"
    "s_cbranch_scc0 .Lk_RL_kv3\n"
    "s_branch .Lk_B_kv3\n"
    ".Lk_X_3:\n"
    "v_mfma_f32_32x32x16_bf16 v[240:255], v[204:207], v[116:119], v[240:255]\n"
    "s_waitcnt vmcnt(0)\n"
    "s_nop 1\n"
    "s_branch .Lk_Latt_end\n"
    ".Lk_Lfallback:\n"
    "s_waitcnt vmcnt(0) lgkmcnt(0)\n"
    "s_cmp_lg_u32 s64, 0\n"
    "s_cbranch_scc1 .Lk_Latt_pub\n"
    "s_mov_b32 s64, 1\n"
    "v_mov_b32 v224, 0\n"
    "v_mov_b32 v225, 0\n"
    "v_mov_b32 v226, 0\n"
    "v_mov_b32 v227, 0\n"
    "v_mov_b32 v228, 0\n"
    "v_mov_b32 v229, 0\n"
    "v_mov_b32 v230, 0\n"
    "v_mov_b32 v231, 0\n"
    "v_mov_b32 v232, 0\n"
    "v_mov_b32 v233, 0\n"
    "v_mov_b32 v234, 0\n"
    "v_mov_b32 v235, 0\n"
    "v_mov_b32 v236, 0\n"
    "v_mov_b32 v237, 0\n"
    "v_mov_b32 v238, 0\n"
    "v_mov_b32 v239, 0\n"
    "v_mov_b32 v80, 0xf1800000\n"
    "s_lshl_b32 s61, s49, 1\n"
    "s_sub_i32 s61, 8, s61\n"
    "s_max_i32 s61, s61, 0\n"
    ".Lk_F00a:\n"
    "s_lshl_b32 s66, s61, 11\n"
    "s_add_u32 s66, s66, s68\n"
    "s_sub_u32 s66, s66, 16384\n"
    "buffer_load_dwordx4 v[128:131], v13, s[20:23], s66 offen sc1\n"
    "buffer_load_dwordx4 v[132:135], v13, s[20:23], s66 offen offset:1024 sc1\n"
    "v_mov_b32 v136, 0x3f803f80\n"
    "v_mov_b32 v137, 0x3f803f80\n"
    "v_mov_b32 v138, 0x3f803f80\n"
    "v_mov_b32 v139, 0x3f803f80\n"
    "s_waitcnt vmcnt(0)\n"
    "v_permlane16_swap_b32 v132, v136\n"
    "v_permlane16_swap_b32 v133, v137\n"
    "v_permlane16_swap_b32 v134, v138\n"
    "v_permlane16_swap_b32 v135, v139\n"
    "v_mfma_f32_32x32x16_bf16 v[64:79], v[128:131], v[24:27], 0\n"
    "s_cmp_eq_u32 s61, 0\n"
    "s_cselect_b32 s58, 1.0, 0\n"
    "s_cmp_eq_u32 s61, 8\n"
    "s_cselect_b32 s58, -1.0, s58\n"
    "s_cselect_b32 s59, 0xf1800000, 0\n"
    "v_mov_b32_e64 v81, s59\n"
    "v_fma_f32 v144, v96, s58, v81\n"
    "v_fma_f32 v145, v97, s58, v81\n"
    "v_fma_f32 v146, v98, s58, v81\n"
    "v_fma_f32 v147, v99, s58, v81\n"
    "v_fma_f32 v148, v100, s58, v81\n"
    "v_fma_f32 v149, v101, s58, v81\n"
    "v_fma_f32 v150, v102, s58, v81\n"
    "v_fma_f32 v151, v103, s58, v81\n"
    "v_fma_f32 v152, v104, s58, v81\n"
    "v_fma_f32 v153, v105, s58, v81\n"
    "v_fma_f32 v154, v106, s58, v81\n"
    "v_fma_f32 v155, v107, s58, v81\n"
    "v_fma_f32 v156, v108, s58, v81\n"
    "v_fma_f32 v157, v109, s58, v81\n"
    "v_fma_f32 v158, v110, s58, v81\n"
    "v_fma_f32 v159, v111, s58, v81\n"
    "v_add_f32 v64, v64, v144\n"
    "v_add_f32 v65, v65, v145\n"
    "v_add_f32 v66, v66, v146\n"
    "v_add_f32 v67, v67, v147\n"
    "v_add_f32 v68, v68, v148\n"
    "v_add_f32 v69, v69, v149\n"
    "v_add_f32 v70, v70, v150\n"
    "v_add_f32 v71, v71, v151\n"
    "v_add_f32 v72, v72, v152\n"
    "v_add_f32 v73, v73, v153\n"
    "v_add_f32 v74, v74, v154\n"
    "v_add_f32 v75, v75, v155\n"
    "v_add_f32 v76, v76, v156\n"
    "v_add_f32 v77, v77, v157\n"
    "v_add_f32 v78, v78, v158\n"
    "v_add_f32 v79, v79, v159\n"
    "v_max3_f32 v80, v80, v64, v65\n"
    "v_max3_f32 v80, v80, v66, v67\n"
    "v_max3_f32 v80, v80, v68, v69\n"
    "v_max3_f32 v80, v80, v70, v71\n"
    "v_max3_f32 v80, v80, v72, v73\n"
    "v_max3_f32 v80, v80, v74, v75\n"
    "v_max3_f32 v80, v80, v76, v77\n"
    "v_max3_f32 v80, v80, v78, v79\n"
    "s_add_u32 s61, s61, 1\n"
    "s_cmp_le_u32 s61, 8\n"
    "s_cbranch_scc1 .Lk_F00a\n"
    "v_mov_b32 v81, v80\n"
    "s_nop 1\n"
    "v_permlane32_swap_b32 v80, v81\n"
    "v_max_f32 v80, v80, v81\n"
    "s_lshl_b32 s61, s49, 1\n"
    "s_sub_i32 s61, 8, s61\n"
    "s_max_i32 s61, s61, 0\n"
    ".Lk_F01a:\n"
    "s_lshl_b32 s66, s61, 11\n"
    "s_add_u32 s66, s66, s68\n"
    "s_sub_u32 s66, s66, 16384\n"
    "buffer_load_dwordx4 v[128:131], v13, s[20:23], s66 offen sc1\n"
    "buffer_load_dwordx4 v[132:135], v13, s[20:23], s66 offen offset:1024 sc1\n"
    "v_mov_b32 v136, 0x3f803f80\n"
    "v_mov_b32 v137, 0x3f803f80\n"
    "v_mov_b32 v138, 0x3f803f80\n"
    "v_mov_b32 v139, 0x3f803f80\n"
    "s_waitcnt vmcnt(0)\n"
    "v_permlane16_swap_b32 v132, v136\n"
    "v_permlane16_swap_b32 v133, v137\n"
    "v_permlane16_swap_b32 v134, v138\n"
    "v_permlane16_swap_b32_e64 v135, v139\n"
    "v_mfma_f32_32x32x16_bf16 v[64:79], v[128:131], v[24:27], 0\n"
    "s_cmp_eq_u32 s61, 0\n"
    "s_cselect_b32 s58, 1.0, 0\n"
    "s_cmp_eq_u32 s61, 8\n"
    "s_cselect_b32 s58, -1.0, s58\n"
    "s_cselect_b32 s59, 0xf1800000, 0\n"
    "v_mov_b32_e64 v81, s59\n"
    "v_fma_f32 v144, v96, s58, v81\n"
    "v_fma_f32 v145, v97, s58, v81\n"
    "v_fma_f32 v146, v98, s58, v81\n"
    "v_fma_f32 v147, v99, s58, v81\n"
    "v_fma_f32 v148, v100, s58, v81\n"
    "v_fma_f32 v149, v101, s58, v81\n"
    "v_fma_f32 v150, v102, s58, v81\n"
    "v_fma_f32 v151, v103, s58, v81\n"
    "v_fma_f32 v152, v104, s58, v81\n"
    "v_fma_f32 v153, v105, s58, v81\n"
    "v_fma_f32 v154, v106, s58, v81\n"
    "v_fma_f32 v155, v107, s58, v81\n"
    "v_fma_f32 v156, v108, s58, v81\n"
    "v_fma_f32 v157, v109, s58, v81\n"
    "v_fma_f32 v158, v110, s58, v81\n"
    "v_fma_f32 v159, v111, s58, v81\n"
    "v_add_f32 v64, v64, v144\n"
    "v_add_f32 v65, v65, v145\n"
    "v_add_f32 v66, v66, v146\n"
    "v_add_f32 v67, v67, v147\n"
    "v_add_f32 v68, v68, v148\n"
    "v_add_f32 v69, v69, v149\n"
    "v_add_f32 v70, v70, v150\n"
    "v_add_f32 v71, v71, v151\n"
    "v_add_f32 v72, v72, v152\n"
    "v_add_f32 v73, v73, v153\n"
    "v_add_f32 v74, v74, v154\n"
    "v_add_f32 v75, v75, v155\n"
    "v_add_f32 v76, v76, v156\n"
    "v_add_f32 v77, v77, v157\n"
    "v_add_f32 v78, v78, v158\n"
    "v_add_f32 v79, v79, v159\n"
    "v_sub_f32 v64, v64, v80\n"
    "v_sub_f32 v65, v65, v80\n"
    "v_sub_f32 v66, v66, v80\n"
    "v_sub_f32 v67, v67, v80\n"
    "v_sub_f32 v68, v68, v80\n"
    "v_sub_f32 v69, v69, v80\n"
    "v_sub_f32 v70, v70, v80\n"
    "v_sub_f32 v71, v71, v80\n"
    "v_sub_f32 v72, v72, v80\n"
    "v_sub_f32 v73, v73, v80\n"
    "v_sub_f32 v74, v74, v80\n"
    "v_sub_f32 v75, v75, v80\n"
    "v_sub_f32 v76, v76, v80\n"
    "v_sub_f32 v77, v77, v80\n"
    "v_sub_f32 v78, v78, v80\n"
    "v_sub_f32 v79, v79, v80\n"
    "v_exp_f32 v64, v64\n"
    "v_exp_f32 v65, v65\n"
    "v_exp_f32 v66, v66\n"
    "v_exp_f32 v67, v67\n"
    "v_exp_f32 v68, v68\n"
    "v_exp_f32 v69, v69\n"
    "v_exp_f32 v70, v70\n"
    "v_exp_f32 v71, v71\n"
    "v_exp_f32 v72, v72\n"
    "v_exp_f32 v73, v73\n"
    "v_exp_f32 v74, v74\n"
    "v_exp_f32 v75, v75\n"
    "v_exp_f32 v76, v76\n"
    "v_exp_f32 v77, v77\n"
    "v_exp_f32 v78, v78\n"
    "v_exp_f32 v79, v79\n"
    "v_cvt_pk_bf16_f32 v112, v64, v65\n"
    "v_cvt_pk_bf16_f32 v113, v66, v67\n"
    "v_cvt_pk_bf16_f32 v114, v68, v69\n"
    "v_cvt_pk_bf16_f32 v115, v70, v71\n"
    "v_cvt_pk_bf16_f32 v116, v72, v73\n"
    "v_cvt_pk_bf16_f32 v117, v74, v75\n"
    "v_cvt_pk_bf16_f32 v118, v76, v77\n"
    "v_cvt_pk_bf16_f32 v119, v78, v79\n"
    "v_mfma_f32_32x32x16_bf16 v[224:239], v[132:135], v[112:115], v[224:239]\n"
    "s_nop 0\n"
    "v_mfma_f32_32x32x16_bf16 v[224:239], v[136:139], v[116:119], v[224:239]\n"
    "s_nop 15\n"
    "s_add_u32 s61, s61, 1\n"
    "s_cmp_le_u32 s61, 8\n"
    "s_cbranch_scc1 .Lk_F01a\n"
    "v_mov_b32 v240, 0\n"
    "v_mov_b32 v241, 0\n"
    "v_mov_b32 v242, 0\n"
    "v_mov_b32 v243, 0\n"
    "v_mov_b32 v244, 0\n"
    "v_mov_b32 v245, 0\n"
    "v_mov_b32 v246, 0\n"
    "v_mov_b32 v247, 0\n"
    "v_mov_b32 v248, 0\n"
    "v_mov_b32 v249, 0\n"
    "v_mov_b32 v250, 0\n"
    "v_mov_b32 v251, 0\n"
    "v_mov_b32 v252, 0\n"
    "v_mov_b32 v253, 0\n"
    "v_mov_b32 v254, 0\n"
    "v_mov_b32_e64 v255, 0\n"
    "v_mov_b32 v80, 0xf1800000\n"
    "s_lshl_b32 s61, s49, 1\n"
    "s_sub_i32 s61, 7, s61\n"
    "s_max_i32 s61, s61, 0\n"
    ".Lk_F10a:\n"
    "s_lshl_b32 s66, s61, 11\n"
    "s_add_u32 s66, s66, s68\n"
    "s_sub_u32 s66, s66, 14336\n"
    "buffer_load_dwordx4 v[128:131], v13, s[20:23], s66 offen sc1\n"
    "buffer_load_dwordx4 v[132:135], v13, s[20:23], s66 offen offset:1024 sc1\n"
    "v_mov_b32 v136, 0x3f803f80\n"
    "v_mov_b32 v137, 0x3f803f80\n"
    "v_mov_b32 v138, 0x3f803f80\n"
    "v_mov_b32 v139, 0x3f803f80\n"
    "s_waitcnt vmcnt(0)\n"
    "v_permlane16_swap_b32 v132, v136\n"
    "v_permlane16_swap_b32 v133, v137\n"
    "v_permlane16_swap_b32 v134, v138\n"
    "v_permlane16_swap_b32 v135, v139\n"
    "v_mfma_f32_32x32x16_bf16 v[64:79], v[128:131], v[28:31], 0\n"
    "s_cmp_eq_u32 s61, 0\n"
    "s_cselect_b32 s58, 1.0, 0\n"
    "s_cmp_eq_u32 s61, 8\n"
    "s_cselect_b32 s58, -1.0, s58\n"
    "s_cselect_b32 s59, 0xf1800000, 0\n"
    "v_mov_b32_e64 v81, s59\n"
    "v_fma_f32 v144, v96, s58, v81\n"
    "v_fma_f32 v145, v97, s58, v81\n"
    "v_fma_f32 v146, v98, s58, v81\n"
    "v_fma_f32 v147, v99, s58, v81\n"
    "v_fma_f32 v148, v100, s58, v81\n"
    "v_fma_f32 v149, v101, s58, v81\n"
    "v_fma_f32 v150, v102, s58, v81\n"
    "v_fma_f32 v151, v103, s58, v81\n"
    "v_fma_f32 v152, v104, s58, v81\n"
    "v_fma_f32 v153, v105, s58, v81\n"
    "v_fma_f32 v154, v106, s58, v81\n"
    "v_fma_f32 v155, v107, s58, v81\n"
    "v_fma_f32 v156, v108, s58, v81\n"
    "v_fma_f32 v157, v109, s58, v81\n"
    "v_fma_f32 v158, v110, s58, v81\n"
    "v_fma_f32 v159, v111, s58, v81\n"
    "v_add_f32 v64, v64, v144\n"
    "v_add_f32 v65, v65, v145\n"
    "v_add_f32 v66, v66, v146\n"
    "v_add_f32 v67, v67, v147\n"
    "v_add_f32 v68, v68, v148\n"
    "v_add_f32 v69, v69, v149\n"
    "v_add_f32 v70, v70, v150\n"
    "v_add_f32 v71, v71, v151\n"
    "v_add_f32 v72, v72, v152\n"
    "v_add_f32 v73, v73, v153\n"
    "v_add_f32 v74, v74, v154\n"
    "v_add_f32 v75, v75, v155\n"
    "v_add_f32 v76, v76, v156\n"
    "v_add_f32 v77, v77, v157\n"
    "v_add_f32 v78, v78, v158\n"
    "v_add_f32 v79, v79, v159\n"
    "v_max3_f32 v80, v80, v64, v65\n"
    "v_max3_f32 v80, v80, v66, v67\n"
    "v_max3_f32 v80, v80, v68, v69\n"
    "v_max3_f32 v80, v80, v70, v71\n"
    "v_max3_f32 v80, v80, v72, v73\n"
    "v_max3_f32 v80, v80, v74, v75\n"
    "v_max3_f32 v80, v80, v76, v77\n"
    "v_max3_f32 v80, v80, v78, v79\n"
    "s_add_u32 s61, s61, 1\n"
    "s_cmp_le_u32 s61, 8\n"
    "s_cbranch_scc1 .Lk_F10a\n"
    "v_mov_b32 v81, v80\n"
    "s_nop 1\n"
    "v_permlane32_swap_b32 v80, v81\n"
    "v_max_f32 v80, v80, v81\n"
    "s_lshl_b32 s61, s49, 1\n"
    "s_sub_i32 s61, 7, s61\n"
    "s_max_i32 s61, s61, 0\n"
    ".Lk_F11a:\n"
    "s_lshl_b32 s66, s61, 11\n"
    "s_add_u32 s66, s66, s68\n"
    "s_sub_u32 s66, s66, 14336\n"
    "buffer_load_dwordx4 v[128:131], v13, s[20:23], s66 offen sc1\n"
    "buffer_load_dwordx4 v[132:135], v13, s[20:23], s66 offen offset:1024 sc1\n"
    "v_mov_b32 v136, 0x3f803f80\n"
    "v_mov_b32 v137, 0x3f803f80\n"
    "v_mov_b32 v138, 0x3f803f80\n"
    "v_mov_b32 v139, 0x3f803f80\n"
    "s_waitcnt vmcnt(0)\n"
    "v_permlane16_swap_b32 v132, v136\n"
    "v_permlane16_swap_b32 v133, v137\n"
    "v_permlane16_swap_b32 v134, v138\n"
    "v_permlane16_swap_b32_e64 v135, v139\n"
    "v_mfma_f32_32x32x16_bf16 v[64:79], v[128:131], v[28:31], 0\n"
    "s_cmp_eq_u32 s61, 0\n"
    "s_cselect_b32 s58, 1.0, 0\n"
    "s_cmp_eq_u32 s61, 8\n"
    "s_cselect_b32 s58, -1.0, s58\n"
    "s_cselect_b32 s59, 0xf1800000, 0\n"
    "v_mov_b32_e64 v81, s59\n"
    "v_fma_f32 v144, v96, s58, v81\n"
    "v_fma_f32 v145, v97, s58, v81\n"
    "v_fma_f32 v146, v98, s58, v81\n"
    "v_fma_f32 v147, v99, s58, v81\n"
    "v_fma_f32 v148, v100, s58, v81\n"
    "v_fma_f32 v149, v101, s58, v81\n"
    "v_fma_f32 v150, v102, s58, v81\n"
    "v_fma_f32 v151, v103, s58, v81\n"
    "v_fma_f32 v152, v104, s58, v81\n"
    "v_fma_f32 v153, v105, s58, v81\n"
    "v_fma_f32 v154, v106, s58, v81\n"
    "v_fma_f32 v155, v107, s58, v81\n"
    "v_fma_f32 v156, v108, s58, v81\n"
    "v_fma_f32 v157, v109, s58, v81\n"
    "v_fma_f32 v158, v110, s58, v81\n"
    "v_fma_f32 v159, v111, s58, v81\n"
    "v_add_f32 v64, v64, v144\n"
    "v_add_f32 v65, v65, v145\n"
    "v_add_f32 v66, v66, v146\n"
    "v_add_f32 v67, v67, v147\n"
    "v_add_f32 v68, v68, v148\n"
    "v_add_f32 v69, v69, v149\n"
    "v_add_f32 v70, v70, v150\n"
    "v_add_f32 v71, v71, v151\n"
    "v_add_f32 v72, v72, v152\n"
    "v_add_f32 v73, v73, v153\n"
    "v_add_f32 v74, v74, v154\n"
    "v_add_f32 v75, v75, v155\n"
    "v_add_f32 v76, v76, v156\n"
    "v_add_f32 v77, v77, v157\n"
    "v_add_f32 v78, v78, v158\n"
    "v_add_f32 v79, v79, v159\n"
    "v_sub_f32 v64, v64, v80\n"
    "v_sub_f32 v65, v65, v80\n"
    "v_sub_f32 v66, v66, v80\n"
    "v_sub_f32 v67, v67, v80\n"
    "v_sub_f32 v68, v68, v80\n"
    "v_sub_f32 v69, v69, v80\n"
    "v_sub_f32 v70, v70, v80\n"
    "v_sub_f32 v71, v71, v80\n"
    "v_sub_f32 v72, v72, v80\n"
    "v_sub_f32 v73, v73, v80\n"
    "v_sub_f32 v74, v74, v80\n"
    "v_sub_f32 v75, v75, v80\n"
    "v_sub_f32 v76, v76, v80\n"
    "v_sub_f32 v77, v77, v80\n"
    "v_sub_f32 v78, v78, v80\n"
    "v_sub_f32 v79, v79, v80\n"
    "v_exp_f32 v64, v64\n"
    "v_exp_f32 v65, v65\n"
    "v_exp_f32 v66, v66\n"
    "v_exp_f32 v67, v67\n"
    "v_exp_f32 v68, v68\n"
    "v_exp_f32 v69, v69\n"
    "v_exp_f32 v70, v70\n"
    "v_exp_f32 v71, v71\n"
    "v_exp_f32 v72, v72\n"
    "v_exp_f32 v73, v73\n"
    "v_exp_f32 v74, v74\n"
    "v_exp_f32 v75, v75\n"
    "v_exp_f32 v76, v76\n"
    "v_exp_f32 v77, v77\n"
    "v_exp_f32 v78, v78\n"
    "v_exp_f32 v79, v79\n"
    "v_cvt_pk_bf16_f32 v112, v64, v65\n"
    "v_cvt_pk_bf16_f32 v113, v66, v67\n"
    "v_cvt_pk_bf16_f32 v114, v68, v69\n"
    "v_cvt_pk_bf16_f32 v115, v70, v71\n"
    "v_cvt_pk_bf16_f32 v116, v72, v73\n"
    "v_cvt_pk_bf16_f32 v117, v74, v75\n"
    "v_cvt_pk_bf16_f32 v118, v76, v77\n"
    "v_cvt_pk_bf16_f32 v119, v78, v79\n"
    "v_mfma_f32_32x32x16_bf16 v[240:255], v[132:135], v[112:115], v[240:255]\n"
    "s_nop 0\n"
    "v_mfma_f32_32x32x16_bf16 v[240:255], v[136:139], v[116:119], v[240:255]\n"
    "s_nop 15\n"
    "s_add_u32 s61, s61, 1\n"
    "s_cmp_le_u32 s61, 8\n"
    "s_cbranch_scc1 .Lk_F11a\n"
    "s_branch .Lk_Latt_norm\n"
    ".Lk_R_o0:\n"
    "s_waitcnt vmcnt(0) lgkmcnt(0)\n"
    "s_mov_b32 s1, 131072\n"
    ".Lk_RL_o0:\n"
    "buffer_load_dwordx4 v[128:131], v13, s[20:23], s66 offen offset:0 sc1\n"
    "buffer_load_dwordx4 v[132:135], v13, s[20:23], s66 offen offset:1024 sc1\n"
    "buffer_load_dwordx4 v[136:139], v13, s[20:23], s66 offen offset:2048 sc1\n"
    "buffer_load_dwordx4 v[140:143], v13, s[20:23], s66 offen offset:3072 sc1\n"
    "buffer_load_dword v160, v61, s[20:23], s64 offen sc1\n"
    "s_waitcnt vmcnt(4)\n"
    "ds_write_b128 v15, v[128:131] offset:15872\n"
    "v_add3_u32 v14, s10, v128, v129\n"
    "v_add3_u32 v14, v14, v130, v131\n"
    "s_waitcnt vmcnt(3)\n"
    "ds_write_b128 v15, v[132:135] offset:16896\n"
    "v_add3_u32 v14, v14, v132, v133\n"
    "v_add3_u32 v14, v14, v134, v135\n"
    "s_waitcnt vmcnt(2)\n"
    "ds_write_b128 v15, v[136:139] offset:17920\n"
    "v_add3_u32 v14, v14, v136, v137\n"
    "v_add3_u32 v14, v14, v138, v139\n"
    "s_waitcnt vmcnt(1)\n"
    "ds_write_b128 v15, v[140:143] offset:18944\n"
    "v_add3_u32 v14, v14, v140, v141\n"
    "v_add3_u32 v14, v14, v142, v143\n"
    "s_waitcnt vmcnt(0)\n"
    "v_sub_u32 v14, v14, v160\n"
    "s_nop 1\n"
    "v_add_u32_dpp v14, v14, v14 quad_perm:[1,0,3,2] row_mask:0xf bank_mask:0xf\n"
    "s_nop 1\n"
    "v_add_u32_dpp v14, v14, v14 quad_perm:[2,3,0,1] row_mask:0xf bank_mask:0xf\n"
    "v_cmp_ne_u32 vcc, 0, v14\n"
    "s_waitcnt vmcnt(0) lgkmcnt(0)\n"
    "s_cbranch_vccz .Lk_B_o0\n"
    "s_sub_u32 s1, s1, 1\n"
    "s_cbranch_scc0 .Lk_RL_o0\n"
    "s_branch .Lk_B_o0\n"
    ".Lk_R_o1:\n"
    "s_waitcnt vmcnt(0) lgkmcnt(0)\n"
    "s_mov_b32 s1, 131072\n"
    ".Lk_RL_o1:\n"
    "buffer_load_dwordx4 v[144:147], v13, s[20:23], s67 offen offset:0 sc1\n"
    "buffer_load_dwordx4 v[148:151], v13, s[20:23], s67 offen offset:1024 sc1\n"
    "buffer_load_dwordx4 v[152:155], v13, s[20:23], s67 offen offset:2048 sc1\n"
    "buffer_load_dwordx4 v[156:159], v13, s[20:23], s67 offen offset:3072 sc1\n"
    "buffer_load_dword v161, v61, s[20:23], s65 offen sc1\n"
    "s_waitcnt vmcnt(4)\n"
    "ds_write_b128 v15, v[144:147] offset:19968\n"
    "v_add3_u32 v14, s10, v144, v145\n"
    "v_add3_u32 v14, v14, v146, v147\n"
    "s_waitcnt vmcnt(3)\n"
    "ds_write_b128 v15, v[148:151] offset:20992\n"
    "v_add3_u32 v14, v14, v148, v149\n"
    "v_add3_u32 v14, v14, v150, v151\n"
    "s_waitcnt vmcnt(2)\n"
    "ds_write_b128 v15, v[152:155] offset:22016\n"
    "v_add3_u32 v14, v14, v152, v153\n"
    "v_add3_u32 v14, v14, v154, v155\n"
    "s_waitcnt vmcnt(1)\n"
    "ds_write_b128 v15, v[156:159] offset:23040\n"
    "v_add3_u32 v14, v14, v156, v157\n"
    "v_add3_u32 v14, v14, v158, v159\n"
    "s_waitcnt vmcnt(0)\n"
    "v_sub_u32_e64 v14, v14, v161\n"
    "s_nop 1\n"
    "v_add_u32_dpp v14, v14, v14 quad_perm:[1,0,3,2] row_mask:0xf bank_mask:0xf\n"
    "s_nop 1\n"
    "v_add_u32_dpp v14, v14, v14 quad_perm:[2,3,0,1] row_mask:0xf bank_mask:0xf\n"
    "v_cmp_ne_u32 vcc, 0, v14\n"
    "s_waitcnt vmcnt(0) lgkmcnt(0)\n"
    "s_cbranch_vccz .Lk_B_o1\n"
    "s_sub_u32 s1, s1, 1\n"
    "s_cbranch_scc0 .Lk_RL_o1\n"
    "s_branch .Lk_B_o1\n"
    ".Lk_Rkeys:\n"
    "s_waitcnt vmcnt(4)\n"
    ".Lk_Rkeys2:\n"
    "s_mov_b64 exec, s[12:13]\n"
    "v_add3_u32 v14, v16, v17, s8\n"
    "v_cmp_ne_u32 s[12:13], v14, v18\n"
    "v_cmp_gt_u32 vcc, v16, v21\n"
    "s_mov_b64 exec, -1\n"
    "s_andn2_b64 vcc, vcc, s[12:13]\n"
    "s_cbranch_scc1 .Lk_Rbest\n"
    "s_cmp_eq_u64 s[12:13], 0\n"
    "s_cbranch_scc1 .Lk_Bkeys\n"
    "s_sub_u32 s1, s1, 1\n"
    "s_cbranch_scc1 .Lk_Bkeys\n"
    "s_mov_b64 exec, s[12:13]\n"
    "buffer_load_dwordx3 v[16:18], v9, s[20:23], s62 offen sc1\n"
    "s_waitcnt vmcnt(0)\n"
    "s_branch .Lk_Rkeys2\n"
    ".Lk_Rbest:\n"
    "s_waitcnt vmcnt(0)\n"
    "s_branch .Lk_Lbest\n"
    ".Lk_R_cf:\n"
    "s_waitcnt vmcnt(0) lgkmcnt(0)\n"
    "s_mov_b32 s1, 131072\n"
    ".Lk_RL_cf:\n"
    "buffer_load_dwordx4 v[24:27], v13, s[20:23], s64 offen sc1\n"
    "buffer_load_dwordx4 v[28:31], v13, s[20:23], s65 offen sc1\n"
    "buffer_load_dword v32, v4, s[20:23], s66 offen sc1\n"
    "buffer_load_dword v33, v4, s[20:23], s67 offen sc1\n"
    "s_waitcnt vmcnt(3)\n"
    "ds_write_b128 v10, v[24:27] offset:0\n"
    "v_add3_u32 v34, v24, v25, s8\n"
    "v_add3_u32 v34, v34, v26, v27\n"
    "s_waitcnt vmcnt(2)\n"
    "ds_write_b128 v10, v[28:31] offset:1088\n"
    "v_add3_u32 v35, v28, v29, s8\n"
    "v_add3_u32 v35, v35, v30, v31\n"
    "s_waitcnt vmcnt(0)\n"
    "v_cmp_ne_u64 vcc, v[32:33], v[34:35]\n"
    "s_waitcnt vmcnt(0) lgkmcnt(0)\n"
    "s_cbranch_vccz .Lk_B_cf\n"
    "s_sub_u32 s1, s1, 1\n"
    "s_cbranch_scc0 .Lk_RL_cf\n"
    "s_branch .Lk_B_cf\n"
    ".Lk_Lend:\n"
    :
    : "s"(__builtin_amdgcn_kernarg_segment_ptr()), "s"(blockIdx.x), "v"(threadIdx.x)
    :
      "v1", "v2", "v3", "v4", "v5", "v6", "v7", "v8", "v9", "v10", "v11", "v12", "v13", "v14", "v15", "v16",
      "v17", "v18", "v19", "v20", "v21", "v22", "v23", "v24", "v25", "v26", "v27", "v28", "v29", "v30", "v31", "v32",
      "v33", "v34", "v35", "v36", "v37", "v38", "v39", "v40", "v41", "v42", "v43", "v44", "v45", "v46", "v47", "v48",
      "v49", "v50", "v51", "v52", "v53", "v54", "v55", "v56", "v57", "v58", "v59", "v60", "v61", "v62", "v63", "v64",
      "v65", "v66", "v67", "v68", "v69", "v70", "v71", "v72", "v73", "v74", "v75", "v76", "v77", "v78", "v79", "v80",
      "v81", "v82", "v83", "v84", "v85", "v86", "v87", "v88", "v89", "v90", "v91", "v92", "v93", "v94", "v95", "v96",
      "v97", "v98", "v99", "v100", "v101", "v102", "v103", "v104", "v105", "v106", "v107", "v108", "v109", "v110", "v111", "v112",
      "v113", "v114", "v115", "v116", "v117", "v118", "v119", "v120", "v121", "v122", "v123", "v124", "v125", "v126", "v127", "v128",
      "v129", "v130", "v131", "v132", "v133", "v134", "v135", "v136", "v137", "v138", "v139", "v140", "v141", "v142", "v143", "v144",
      "v145", "v146", "v147", "v148", "v149", "v150", "v151", "v152", "v153", "v154", "v155", "v156", "v157", "v158", "v159", "v160",
      "v161", "v162", "v163", "v164", "v165", "v166", "v167", "v168", "v169", "v170", "v171", "v172", "v173", "v174", "v175", "v176",
      "v177", "v178", "v179", "v180", "v181", "v182", "v183", "v184", "v185", "v186", "v187", "v188", "v189", "v190", "v191", "v192",
      "v193", "v194", "v195", "v196", "v197", "v198", "v199", "v200", "v201", "v202", "v203", "v204", "v205", "v206", "v207", "v208",
      "v209", "v210", "v211", "v212", "v213", "v214", "v215", "v216", "v217", "v218", "v219", "v220", "v221", "v222", "v223", "v224",
      "v225", "v226", "v227", "v228", "v229", "v230", "v231", "v232", "v233", "v234", "v235", "v236", "v237", "v238", "v239", "v240",
      "v241", "v242", "v243", "v244", "v245", "v246", "v247", "v248", "v249", "v250", "v251", "v252", "v253", "v254", "v255", "s3",
      "s4", "s5", "s6", "s7", "s8", "s9", "s10", "s11", "s12", "s13", "s14", "s15", "s16", "s17", "s18", "s19",
      "s20", "s21", "s22", "s23", "s24", "s25", "s26", "s27", "s28", "s29", "s30", "s31", "s33", "s34", "s35", "s36",
      "s37", "s38", "s39", "s40", "s41", "s42", "s43", "s44", "s45", "s46", "s47", "s48", "s49", "s50", "s51", "s52",
      "s53", "s54", "s55", "s56", "s57", "s58", "s59", "s60", "s61", "s62", "s63", "s64", "s65", "s66", "s67", "s68",
      "s69", "s70", "s71", "s72", "s73", "s74", "s75", "s76", "s77", "s78", "s79", "s80", "s81", "s82", "s83", "s84",
      "s85", "s86", "s87", "s88", "s89", "s90", "s91", "s92", "s93", "s94", "s95", "s96", "s97", "s98", "s99", "vcc",
      "scc", "memory"
  );
}

extern "C" void kernel_launch(void* const* d_in, const int* in_sizes, int n_in, void* d_out, int out_size,
                              void* d_ws, size_t ws_size, hipStream_t stream) {
  const int order[11] = {9, 8, 0, 1, -1, 2, 4, -2, 5, 6, 7};
  Args a;
  for (int i = 0; i < 11; ++i) {
    const int k = order[i];
    a.b[i].p = k >= 0 ? d_in[k] : (k == -1 ? d_ws : d_out);
    a.b[i].bytes = k >= 0 ? (unsigned)in_sizes[k] * 2u : (k == -1 ? 12582912u : (unsigned)out_size * 2u);
    a.b[i].fmt = 0x00020000u;
  }
  (void)hipFuncSetAttribute((const void*)fused_block, hipFuncAttributeMaxDynamicSharedMemorySize, 163840);
  fused_block<<<256, 512, 163840, stream>>>(a);
}
```
